```python
import torch

def _softmax(x, axis=-1):
    return torch.softmax(x, dim=axis)
def _gelu(x):
    return torch.nn.functional.gelu(x, approximate="tanh")
def _silu(x):
    return torch.nn.functional.silu(x)
def _sigmoid(x):
    return torch.sigmoid(x)
def _layernorm(x, axis=-1, eps=1e-5):
    m = x.mean(dim=axis, keepdim=True)
    v = x.var(dim=axis, keepdim=True, unbiased=False)
    return (x - m) / torch.sqrt(v + eps)
def _scatter_add(tgt, idx, src, axis=0):
    return tgt.clone().index_add_(axis, idx, src)
def _segment_sum(src, seg, n):
    out = torch.zeros((n,) + src.shape[1:], dtype=src.dtype)
    return out.index_add_(0, seg, src)
def _pad(x, pw):
    flat = tuple(v for pair in reversed(pw) for v in pair)
    return torch.nn.functional.pad(x, flat)

kernel_name = 'gated_mix__spectral_fnet__glu_gated_glu__prompt'

def setup_inputs():

    _w = (lambda a: (lambda t: t.to(torch.bfloat16) if t.is_floating_point() else t)(torch.as_tensor(a)))
    def _A_setup():
        B, S, D = 1, 2048, 512
        ns = torch.arange(S)[:, None] * torch.arange(S)[None, :]
        nd = torch.arange(D)[:, None] * torch.arange(D)[None, :]
        Ws_re = torch.cos(-2.0 * torch.pi * ns / S).to(float)
        Ws_im = torch.sin(-2.0 * torch.pi * ns / S).to(float)
        Wd_re = torch.cos(-2.0 * torch.pi * nd / D).to(float)
        Wd_im = torch.sin(-2.0 * torch.pi * nd / D).to(float)
        return {
            "x": _w((lambda *s: torch.randn(*s, dtype=torch.bfloat16) * 0.05)(B, S, D)),
            "dft_seq_re": _w(Ws_re),
            "dft_seq_im": _w(Ws_im),
            "dft_hid_re": _w(Wd_re),
            "dft_hid_im": _w(Wd_im),
        }
    _A_in = _A_setup()

    def _B_setup():
        B, S, D, F = 1, 2048, 512, 1792
        return {
            "x": _w((lambda *s: torch.randn(*s, dtype=torch.bfloat16) * 0.05)(B, S, D)),
            "w_gate": _w((lambda *s: torch.randn(*s, dtype=torch.bfloat16) * 1.6)(D, F)),
            "w_up": _w((lambda *s: torch.randn(*s, dtype=torch.bfloat16) * 0.8)(D, F)),
            "w_down": _w((lambda *s: torch.randn(*s, dtype=torch.bfloat16) * 0.8)(F, D)),
        }
    _B_in = _B_setup()

    return {
        "A_x": _A_in["x"],
        "A_dft_seq_re": _A_in["dft_seq_re"],
        "A_dft_seq_im": _A_in["dft_seq_im"],
        "A_dft_hid_re": _A_in["dft_hid_re"],
        "A_dft_hid_im": _A_in["dft_hid_im"],
        "B_x": _B_in["x"],
        "B_w_gate": _B_in["w_gate"],
        "B_w_up": _B_in["w_up"],
        "B_w_down": _B_in["w_down"],
        "gate_w": _w((lambda *s: torch.randn(*s, dtype=torch.bfloat16) * 0.05)(512) * 16.0),
    }

def reference(A_x, A_dft_seq_re, A_dft_seq_im, A_dft_hid_re, A_dft_hid_im, B_x, B_w_gate, B_w_up, B_w_down, gate_w):
    def _A_ref(x, dft_seq_re, dft_seq_im, dft_hid_re, dft_hid_im):
        a = torch.einsum("bsd,ed->bse", x, dft_hid_re)
        b = torch.einsum("bsd,ed->bse", x, dft_hid_im)
        y = torch.einsum("ts,bse->bte", dft_seq_re, a) - torch.einsum("ts,bse->bte", dft_seq_im, b)
        return y

    def _B_ref(x, w_gate, w_up, w_down):
        g = torch.einsum("bsd,df->bsf", x, w_gate)
        u = torch.einsum("bsd,df->bsf", x, w_up)
        h = _sigmoid(g) * u
        return torch.einsum("bsf,fd->bsd", h, w_down)

    x = A_x
    g = _sigmoid((x * gate_w).sum(-1, keepdims=True))
    a = _A_ref(x, A_dft_seq_re, A_dft_seq_im, A_dft_hid_re, A_dft_hid_im)
    b = _B_ref(x, B_w_gate, B_w_up, B_w_down)
    return g * a + (1 - g) * b
```

```cpp
#include <hip/hip_runtime.h>
#include <hip/hip_bf16.h>
typedef __hip_bfloat16 bf16;
__device__ __forceinline__ float b2f(bf16 v){ return __bfloat162float(v); }
constexpr int T=2048, D=512, F=1792;
__global__ void k_ab(const bf16* x, const bf16* Hr, const bf16* Hi, bf16* ab){
  int e = blockIdx.x*256+threadIdx.x; int s = blockIdx.y;
  const bf16* H = e<512? Hr + (size_t)e*D : Hi + (size_t)(e-512)*D;
  float acc=0; for(int d=0; d<D; ++d) acc += b2f(x[s*D+d])*b2f(H[d]);
  ab[s*1024+e] = __float2bfloat16(acc);
}
__global__ void k_h(const bf16* x, const bf16* Wg, const bf16* Wu, bf16* h){
  int f = blockIdx.x*256+threadIdx.x; int t = blockIdx.y;
  float g=0,u=0; for(int d=0; d<D; ++d){ float xv=b2f(x[t*D+d]); g+=xv*b2f(Wg[d*F+f]); u+=xv*b2f(Wu[d*F+f]); }
  h[t*F+f] = __float2bfloat16(u/(1.f+__expf(-g)));
}
__global__ void k_out(const bf16* x, const bf16* gw, const bf16* Cr, const bf16* Ci, const bf16* ab, const bf16* h, const bf16* Wd, bf16* out){
  int e = blockIdx.x*256+threadIdx.x; int t = blockIdx.y;
  float z=0; for(int d=0; d<D; ++d) z += b2f(x[t*D+d])*b2f(gw[d]);
  float g = 1.f/(1.f+__expf(-z));
  float y=0; for(int s=0; s<T; ++s){ y += b2f(Cr[(size_t)t*T+s])*b2f(ab[s*1024+e]) - b2f(Ci[(size_t)t*T+s])*b2f(ab[s*1024+512+e]); }
  float w=0; for(int f=0; f<F; ++f) w += b2f(h[t*F+f])*b2f(Wd[f*D+e]);
  out[t*D+e] = __float2bfloat16(g*y+(1.f-g)*w);
}
extern "C" void kernel_launch(void* const* d_in, const int* in_sizes, int n_in,
                              void* d_out, int out_size, void* d_ws, size_t ws_size,
                              hipStream_t stream) {
  const bf16* x=(const bf16*)d_in[0]; const bf16* Cr=(const bf16*)d_in[1]; const bf16* Ci=(const bf16*)d_in[2];
  const bf16* Hr=(const bf16*)d_in[3]; const bf16* Hi=(const bf16*)d_in[4];
  const bf16* Wg=(const bf16*)d_in[6]; const bf16* Wu=(const bf16*)d_in[7]; const bf16* Wd=(const bf16*)d_in[8]; const bf16* gw=(const bf16*)d_in[9];
  bf16* ab=(bf16*)d_ws; bf16* h=ab+T*1024;
  k_ab<<<dim3(4,T),256,0,stream>>>(x,Hr,Hi,ab);
  k_h<<<dim3(7,T),256,0,stream>>>(x,Wg,Wu,h);
  k_out<<<dim3(2,T),256,0,stream>>>(x,gw,Cr,Ci,ab,h,Wd,(bf16*)d_out);
}
```

```cpp
#include <hip/hip_runtime.h>
#include <stdint.h>

#define LAS __attribute__((address_space(3)))
#define GAS __attribute__((address_space(1)))
#define DI __device__ __forceinline__
typedef unsigned short u16;
typedef __bf16 bf16x8 __attribute__((ext_vector_type(8)));
typedef __bf16 bf16x4 __attribute__((ext_vector_type(4)));
typedef __bf16 bf16x2 __attribute__((ext_vector_type(2)));
typedef float f32x4 __attribute__((ext_vector_type(4)));
typedef unsigned u32x4 __attribute__((ext_vector_type(4)));

constexpr int T_ = 2048, D_ = 512, F_ = 1792;
constexpr size_t WS_FLAG = 0;
constexpr size_t WS_ABF = 1u << 20;
constexpr size_t WS_HF = 8u << 20;
constexpr size_t WS_PART = 16u << 20;
constexpr int L_GW = 147456, L_Z = L_GW + 2048, L_TCH = L_GW + 4096, L_SIZE = 163840;

#define MFMA(a, b, c) __builtin_amdgcn_mfma_f32_16x16x32_bf16((a), (b), (c), 0, 0, 0)
#define WAIT_V(n) asm volatile("s_waitcnt vmcnt(%0)" ::"n"(n) : "memory")
#define BAR() __builtin_amdgcn_s_barrier()
#define SCHED() __builtin_amdgcn_sched_barrier(0)
#define DSR(dst, addr, off) asm volatile("ds_read_b128 %0, %1 offset:%2" : "=v"(dst) : "v"(addr), "i"(off))
#define DSR32(dst, addr, off) asm volatile("ds_read_b32 %0, %1 offset:%2" : "=v"(dst) : "v"(addr), "i"(off))
#define DTR(dst, addr, off) asm volatile("ds_read_b64_tr_b16 %0, %1 offset:%2" : "=v"(dst) : "v"(addr), "i"(off))
#define ADV(x, d) asm volatile("v_add_u32 %0, %1, %0" : "+v"(x) : "s"(d))
#define WL(n) asm volatile("s_waitcnt lgkmcnt(%0)" ::"n"(n))
#define TIE1(a) asm volatile("" : "+v"(a))
#define TIE2(a, b) asm volatile("" : "+v"(a), "+v"(b))
#define TIE4(a, b, c, d) asm volatile("" : "+v"(a), "+v"(b), "+v"(c), "+v"(d))

#define STAMP(i)

struct TF { bf16x4 lo, hi; };
DI bf16x8 cat(const TF& f) { return __builtin_shufflevector(f.lo, f.hi, 0, 1, 2, 3, 4, 5, 6, 7); }
DI unsigned pk_bf16(float lo, float hi) { bf16x2 v; v[0] = (__bf16)lo; v[1] = (__bf16)hi; return __builtin_bit_cast(unsigned, v); }
DI __amdgpu_buffer_rsrc_t mk_rsrc(const void* p, unsigned bytes) { return __builtin_amdgcn_make_buffer_rsrc((void*)p, 0, bytes, 0x00020000); }
DI void glds(__amdgpu_buffer_rsrc_t r, LAS char* dst, int voff, int soff) {
  __builtin_amdgcn_raw_ptr_buffer_load_lds(r, (LAS void*)dst, 16, voff, soff, 0, 0);
}
DI void touch(__amdgpu_buffer_rsrc_t r, LAS char* dst, int voff) {
  __builtin_amdgcn_raw_ptr_buffer_load_lds(r, (LAS void*)dst, 4, voff, 0, 0, 0);
}
DI void st_wt(u32x4 v, __amdgpu_buffer_rsrc_t r, int voff, int soff) { __builtin_amdgcn_raw_buffer_store_b128(v, r, voff, soff, 16); }
template <int N> struct IC { static constexpr int v = N; };

__global__ __launch_bounds__(512, 2) void fused(const u16* __restrict__ x, const u16* __restrict__ Cr, const u16* __restrict__ Ci,
                                                const u16* __restrict__ Hr, const u16* __restrict__ Hi, const u16* __restrict__ Wg,
                                                const u16* __restrict__ Wu, const u16* __restrict__ Wd, const u16* __restrict__ gwp,
                                                char* __restrict__ ws, u16* __restrict__ out) {
  __shared__ __attribute__((aligned(1024))) char smem_[L_SIZE];
  LAS char* smem = (LAS char*)smem_;
  const int id = blockIdx.x, xcd = id & 7, idx = id >> 3;
  const int rb = (xcd & 1) * 8 + (idx >> 2), j = idx & 3, s = xcd >> 1, cb = 4 * s + j, nb = j;
  const int wid = __builtin_amdgcn_readfirstlane(threadIdx.x >> 6), lane = threadIdx.x & 63;
  STAMP(0);

  if (wid >= 4) {
    const int lw = wid - 4, hand = lw >> 1, l1 = lw & 1;
    const int r8 = lane >> 3, c8 = lane & 7, row4 = lane >> 4, ph = lane & 15;
    const int sw0 = (c8 ^ (r8 >> 1)) * 16, sw1 = sw0 ^ 64;
    const __amdgpu_buffer_rsrc_t rx = mk_rsrc(x + (size_t)rb * 128 * D_, 128 * 1024);
    {
      const __amdgpu_buffer_rsrc_t rh = mk_rsrc((cb < 8 ? Hr : Hi) + (size_t)(cb & 7) * 64 * D_, 64 * 1024);
      const int rx_ = (8 * (r8 >> 2) + (r8 & 3)) * 1024;
      const int rh_ = (32 * (lw >> 1) + 8 * (r8 >> 2) + 4 * (l1 ^ (r8 >> 2)) + (r8 & 3)) * 1024;
      const int vxa[2] = {rx_ + sw0, rx_ + sw1}, vha[2] = {rh_ + sw0, rh_ + 16384 + sw1};
      auto issueA = [&](int t) {
        LAS char* sb = smem + (t % 6) * 24576;
#pragma unroll
        for (int i = 0; i < 4; ++i) glds(rx, sb + (4 * lw + i) * 1024, vxa[i & 1], (32 * lw + 16 * (i & 1) + 4 * (i >> 1)) * 1024 + t * 128);
#pragma unroll
        for (int i = 0; i < 2; ++i) glds(rh, sb + 16384 + (2 * lw + i) * 1024, vha[i], t * 128);
      };
      if (lw == 0) glds(mk_rsrc(gwp, 1024), smem + L_GW, lane * 16, 0);
      issueA(0); issueA(1); issueA(2);
      WAIT_V(12); STAMP(1); BAR();
      issueA(3); issueA(4); issueA(5);
      WAIT_V(24); BAR(); issueA(6);
      WAIT_V(24); BAR(); issueA(7);
    }
    const int tbx0 = (2 * lw & 4) | ((2 * lw & 3) ^ s), tbx1 = tbx0 ^ 1;
    {
      const __amdgpu_buffer_rsrc_t rw = mk_rsrc(hand ? Wu : Wg, D_ * F_ * 2);
      const int vxb[2] = {r8 * 1024 + sw0, r8 * 1024 + 8192 + sw1};
      int vw[2];
#pragma unroll
      for (int hb = 0; hb < 2; ++hb) {
        const int v = ph ^ ((row4 & 1) | ((row4 >> 1) << 2) | (hb << 3));
        int gu = v + 2 * (cb & 1); gu = gu >= 14 ? gu - 14 : gu;
        vw[hb] = v < 14 ? row4 * (F_ * 2) + cb * 224 + gu * 16 : (int)0x80000000;
      }
      auto issueB = [&](int t, int reg) {
        LAS char* sb = smem + reg * 49152;
#pragma unroll
        for (int i = 0; i < 4; ++i) glds(rx, sb + (4 * lw + i) * 1024, vxb[i & 1], ((i >> 1) ? tbx1 : tbx0) * 16384 + t * 128);
#pragma unroll
        for (int i = 0; i < 8; ++i) glds(rw, sb + 16384 + (8 * lw + i) * 1024, vw[(i >> 1) & 1], (64 * t + 32 * l1 + 4 * i) * (F_ * 2));
      };
      WAIT_V(24); BAR();
      WAIT_V(18); BAR(); issueB(0, 1);
      WAIT_V(24); BAR();
      WAIT_V(18); BAR(); issueB(1, 2);
      WAIT_V(24); BAR();
      int reg = 0;
#pragma unroll 1
      for (int t = 2; t < 7; ++t) {
        WAIT_V(12); BAR(); issueB(t, reg);
        reg = reg == 2 ? 0 : reg + 1;
      }
      WAIT_V(12); BAR();
      unsigned pv = 1u;
      const int fa = (((4 * s + ((lane >> 2) & 3)) * 16 + 2 * nb + ((lane >> 4) & 1) + 8 * (lane >> 5)) * 16 + (lane & 3)) * 4;
      if (hand) asm volatile("global_load_dword %0, %1, %2 sc1" : "=v"(pv) : "v"(fa), "s"(ws) : "memory");
      issueB(7, 2);
      asm volatile("s_waitcnt vmcnt(12)" : "+v"(pv)::"memory"); BAR();
      if (hand) {
        STAMP(2);
        for (unsigned spins = 0; !__all(pv != 0u) && spins < (1u << 16); ++spins) {
          __builtin_amdgcn_s_sleep(1);
          pv = __hip_atomic_load((GAS unsigned*)(ws + fa), __ATOMIC_RELAXED, __HIP_MEMORY_SCOPE_AGENT);
        }
        asm volatile("buffer_inv sc1" ::: "memory");
        STAMP(3);
      }
    }
    int slot = 0;
    {
      const size_t coff = (size_t)rb * 128 * T_ + s * 512;
      const char* abf = ws + WS_ABF + (size_t)(s * 4 + nb) * 262144;
      const unsigned n0 = hand ? 262144u : (unsigned)(((size_t)T_ * T_ - coff) * 2), n1 = hand ? 131072u : n0;
      const __amdgpu_buffer_rsrc_t r0 = mk_rsrc(hand ? abf : (const char*)(Cr + coff), n0);
      const __amdgpu_buffer_rsrc_t r1 = mk_rsrc(hand ? abf + 131072 : (const char*)(Ci + coff), n1);
      const int vo[2] = {hand ? lane * 16 : r8 * (T_ * 2) + sw0, hand ? lane * 16 : r8 * (T_ * 2) + sw1};
      const int inc = hand ? 16384 : 128;
      int ro[8];
#pragma unroll
      for (int i = 0; i < 8; ++i) {
        const int P = 4 * l1 + (i >> 1), tb = (P & 4) | ((P & 3) ^ s);
        ro[i] = hand ? (8 * l1 + i) * 1024 : (16 * tb + 8 * (i & 1)) * (T_ * 2);
      }
      auto issueC = [&](int t) {
        LAS char* sb = smem + slot * 32768 + lw * 8192;
        const __amdgpu_buffer_rsrc_t r = t < 8 ? r0 : r1;
#pragma unroll
        for (int i = 0; i < 8; ++i) glds(r, sb + i * 1024, vo[i & 1], ro[i] + (t & 7) * inc);
        slot = slot == 4 ? 0 : slot + 1;
      };
      issueC(0);
      WAIT_V(8); BAR(); issueC(1); issueC(2);
      WAIT_V(16); BAR(); issueC(3); issueC(4);
#pragma unroll 1
      for (int t = 5; t < 15; ++t) { WAIT_V(24); BAR(); issueC(t); }
      WAIT_V(24); BAR();
      unsigned pv = 1u;
      const int pl = lane < 28 ? lane : 0;
      const int fb = (pl < 16 ? (rb * 16 + 4 * s + (pl >> 2)) * 16 + 4 + (pl & 3) : (rb * 16 + 4 * (s ^ ((pl >> 2) - 3)) + j) * 16 + (pl & 3)) * 4;
      if (hand) asm volatile("global_load_dword %0, %1, %2 sc1" : "=v"(pv) : "v"(fb), "s"(ws) : "memory");
      issueC(15);
      WAIT_V(24); BAR();
      if (hand) {
        asm volatile("s_waitcnt vmcnt(8)" : "+v"(pv)::"memory");
        STAMP(4);
        for (unsigned spins = 0; !__all(pv != 0u) && spins < (1u << 16); ++spins) {
          __builtin_amdgcn_s_sleep(1);
          pv = __hip_atomic_load((GAS unsigned*)(ws + fb), __ATOMIC_RELAXED, __HIP_MEMORY_SCOPE_AGENT);
        }
        asm volatile("buffer_inv sc1" ::: "memory");
        STAMP(5);
      }
    }
    {
      const size_t woff = (size_t)448 * s * D_ + nb * 128;
      const __amdgpu_buffer_rsrc_t r = mk_rsrc(hand ? ws + WS_HF + (size_t)(rb * 4 + s) * 114688 : (const char*)(Wd + woff),
                                               hand ? 114688u : (unsigned)(((size_t)F_ * D_ - woff) * 2));
      const int u0 = ph ^ ((row4 & 1) | ((row4 >> 1) << 2));
      const int vo[2] = {hand ? lane * 16 : row4 * 1024 + u0 * 16, hand ? lane * 16 : row4 * 1024 + (u0 ^ 8) * 16};
      const int inc = hand ? 16384 : 65536;
      int ro[8];
#pragma unroll
      for (int i = 0; i < 8; ++i) ro[i] = hand ? l1 * 8192 + ((i & 4) | ((i & 3) ^ s)) * 1024 : (32 * l1 + 4 * i) * 1024;
      auto issueD = [&](int t) {
        LAS char* sb = smem + slot * 32768 + (lw ^ 2) * 8192;
#pragma unroll
        for (int i = 0; i < 8; ++i) glds(r, sb + i * 1024, vo[(i >> 1) & 1], ro[i] + t * inc);
        slot = slot == 4 ? 0 : slot + 1;
      };
      issueD(0);
#pragma unroll 1
      for (int t = 1; t < 7; ++t) { WAIT_V(24); BAR(); issueD(t); }
      WAIT_V(24); BAR();
      WAIT_V(16); BAR();
      WAIT_V(8); BAR();
      WAIT_V(0); STAMP(6); BAR();
    }
    return;
  }

  const int w = wid, r2 = w >> 1, c2 = w & 1;
  const int rho = lane & 15, q = lane >> 4, r4 = rho >> 2, p = lane & 3;
  const unsigned sb = (unsigned)(size_t)smem;
  const float L2E = 1.4426950408889634f;
  const int tile = rb * 4 + nb;
  const __amdgpu_buffer_rsrc_t rp = mk_rsrc(ws + WS_PART + (size_t)tile * 98304, 98304);
  const __amdgpu_buffer_rsrc_t rcr = mk_rsrc(Cr + (size_t)rb * 128 * T_ + s * 512, 127 * T_ * 2 + 1024);
  const int vtc = (32 * j + (lane & 31)) * (T_ * 2) + (2 * w + (lane >> 5)) * 128;
  {
    touch(mk_rsrc(x + (size_t)rb * 128 * D_, 128 * 1024), smem + L_TCH, (32 * j + 8 * w + (lane >> 3)) * 1024 + (lane & 7) * 128);
    const int vt = w == 0 ? (8 * (rb & 7) + (lane >> 3)) * 1024 + (lane & 7) * 128 : (64 * (rb & 7) + lane) * (F_ * 2) + cb * 224 + (w == 1 ? 0 : w == 2 ? 112 : 220);
    const u16* hp = (cb < 8 ? Hr : Hi) + (size_t)(cb & 7) * 64 * D_;
    const unsigned nbt = w == 0 ? 65536u : (unsigned)(D_ * F_ * 2);
    touch(mk_rsrc(w == 0 ? hp : Wg, nbt), smem + L_TCH, vt);
    touch(mk_rsrc(w == 0 ? hp : Wu, nbt), smem + L_TCH, vt);
    const u32x4 e = {0xffffffffu, 0xffffffffu, 0xffffffffu, 0xffffffffu};
#pragma unroll
    for (int i = 0; i < 3; ++i) { st_wt(e, rp, lane * 16, ((s * 3 + i) * 4 + w) * 2048); st_wt(e, rp, lane * 16 + 1024, ((s * 3 + i) * 4 + w) * 2048); }
  }
  const unsigned rowp = rho * 128 + ((q ^ (rho >> 1)) * 16);
  const unsigned swz = (r4 & 1) | ((r4 >> 1) << 2) | ((q & 1) << 3);

  unsigned xB00 = sb + 49152 + (4 * r2 + 2 * c2) * 2048 + rowp, xB01 = xB00 ^ 4096, xB10 = xB00 ^ 64, xB11 = xB01 ^ 64;
  const unsigned wrow = sb + 49152 + 16384 + (8 * q + r4) * 256;
  unsigned wO0 = wrow + (((4 * c2 + p) ^ swz) * 16) + (p & 1) * 8, wO1 = wO0 ^ 8;
  unsigned wP0 = wrow + (((8 + p) ^ swz) * 16) + (p & 1) * 8, wP1 = wP0 ^ 8;
  const unsigned gam = (0x2130u >> (4 * p)) & 3u;
  unsigned wS = wrow + (((12 + (gam >> 1)) ^ swz) * 16) + (gam & 1) * 8;
  bf16x8 xc[4], xn[4];
  TF O0g, O0u, O1g, O1u, P0g, P0u, P1g, P1u, Sg, Su;

  {
    struct FA { bf16x8 x0, x1, h0, h1, h2, h3, g; };
    FA f0, f1;
    unsigned xA0 = sb + 4096 * w + rowp, xA1 = xA0 ^ 64, hA0 = sb + 16384 + rowp, hA1 = hA0 ^ 64, gA = sb + L_GW + q * 16;
    f32x4 ac[2][4] = {}, z0 = {}, z1 = {};
#define KA_HEAD(fc) WL(0); TIE4(fc.x0, fc.x1, fc.h0, fc.h1); TIE2(fc.h2, fc.h3); TIE1(fc.g); SCHED();
#define KA(fc, G0, G1, G2, G3, G4, G5, G6, G7, G8) \
    ac[0][0] = MFMA(fc.x0, fc.h0, ac[0][0]); SCHED(); G0; SCHED(); \
    ac[1][0] = MFMA(fc.x1, fc.h0, ac[1][0]); SCHED(); G1; SCHED(); \
    ac[0][1] = MFMA(fc.x0, fc.h1, ac[0][1]); SCHED(); G2; SCHED(); \
    ac[1][1] = MFMA(fc.x1, fc.h1, ac[1][1]); SCHED(); G3; SCHED(); \
    ac[0][2] = MFMA(fc.x0, fc.h2, ac[0][2]); SCHED(); G4; SCHED(); \
    ac[1][2] = MFMA(fc.x1, fc.h2, ac[1][2]); SCHED(); G5; SCHED(); \
    ac[0][3] = MFMA(fc.x0, fc.h3, ac[0][3]); SCHED(); G6; SCHED(); \
    ac[1][3] = MFMA(fc.x1, fc.h3, ac[1][3]); SCHED(); G7; SCHED(); \
    z0 = MFMA(fc.x0, fc.g, z0); SCHED(); G8; SCHED(); \
    z1 = MFMA(fc.x1, fc.g, z1); SCHED();
#define KA_RD(fn, XA, HA, GO) DSR(fn.x0, XA, 0), DSR(fn.h0, HA, 0), DSR(fn.x1, XA, 2048), DSR(fn.h1, HA, 2048), DSR(fn.h2, HA, 4096), DSR(fn.h3, HA, 6144), DSR(fn.g, gA, GO)
#define KA_X(fc, ...) KA(fc, __VA_ARGS__)
    BAR();
    STAMP(1);
    touch(rcr, smem + L_TCH, vtc);
    DSR(f0.x0, xA0, 0); DSR(f0.h0, hA0, 0); DSR(f0.x1, xA0, 2048); DSR(f0.h1, hA0, 2048); DSR(f0.h2, hA0, 4096); DSR(f0.h3, hA0, 6144); DSR(f0.g, gA, 0);
#pragma unroll 1
    for (int t = 0; t < 7; ++t) {
      const int dl = t == 5 ? -5 * 24576 : 24576;
      KA_HEAD(f0);
      KA_X(f0, KA_RD(f1, xA1, hA1, 64), ADV(xA0, dl); ADV(hA0, dl), ADV(xA1, dl); ADV(hA1, dl));
      KA_HEAD(f1);
      BAR();
      KA_X(f1, KA_RD(f0, xA0, hA0, 128), ADV(gA, 128), );
    }
    KA_HEAD(f0);
    KA_X(f0, KA_RD(f1, xA1, hA1, 64), , );
    KA_HEAD(f1);
    BAR();
    KA(f1, DTR(O0g.lo, wO0, 0); DTR(O0g.hi, wO0, 1024); DSR(xc[0], xB00, 0),
           DSR(xc[1], xB00, 2048); DTR(O0u.lo, wO0, 16384); DTR(O0u.hi, wO0, 17408),
           DSR(xc[2], xB01, 0); DSR(xc[3], xB01, 2048),
           DTR(O1g.lo, wO1, 0); DTR(O1g.hi, wO1, 1024); DTR(O1u.lo, wO1, 16384),
           DTR(O1u.hi, wO1, 17408); DTR(P0g.lo, wP0, 0); DTR(P0g.hi, wP0, 1024),
           DTR(P0u.lo, wP0, 16384); DTR(P0u.hi, wP0, 17408); DTR(P1g.lo, wP1, 0),
           DTR(P1g.hi, wP1, 1024); DTR(P1u.lo, wP1, 16384); DTR(P1u.hi, wP1, 17408),
           DTR(Sg.lo, wS, 0); DTR(Sg.hi, wS, 1024),
           DTR(Su.lo, wS, 16384); DTR(Su.hi, wS, 17408));
    STAMP(2);
    const unsigned sg = cb < 8 ? 0u : 0x80008000u;
    const __amdgpu_buffer_rsrc_t ra = mk_rsrc(ws + WS_ABF + (size_t)(((rb >> 2) * 4 + ((cb & 7) >> 1)) * 16 + (cb & 8) + 2 * (rb & 3) + r2) * 16384 + (c2 * 8 + 4 * (cb & 1)) * 1024, 4096);
#pragma unroll
    for (int e = 0; e < 4; ++e) {
      u32x4 v = {pk_bf16(ac[0][e][0], ac[0][e][1]) ^ sg, pk_bf16(ac[0][e][2], ac[0][e][3]) ^ sg, pk_bf16(ac[1][e][0], ac[1][e][1]) ^ sg, pk_bf16(ac[1][e][2], ac[1][e][3]) ^ sg};
      st_wt(v, ra, lane * 16, e * 1024);
    }
    touch(mk_rsrc(Ci + (size_t)rb * 128 * T_ + s * 512, 127 * T_ * 2 + 1024), smem + L_TCH, vtc);
    touch(mk_rsrc(Wd + (size_t)448 * s * D_ + nb * 128, 447 * 1024 + 256), smem + L_TCH, (56 * (rb & 7) + (lane < 56 ? lane : 55)) * 1024 + c2 * 128);
    const unsigned zw = sb + L_Z + (32 * w + 8 * q + 4 * (rho & 1)) * 4;
    const f32x4 zs = (rho & 1) ? z1 : z0;
    if (rho < 2) asm volatile("ds_write_b128 %0, %1" ::"v"(zw), "v"(zs));
    SCHED();
  }

  f32x4 gO0[4] = {}, uO0[4] = {}, gO1[4] = {}, uO1[4] = {}, gP0[2] = {}, uP0[2] = {}, gP1[2] = {}, uP1[2] = {}, gS[2] = {}, uS[2] = {};
  float cz[4];
  bf16x8 fa[4], fb0[4], fb1[4];
  unsigned aC0 = sb + 4 * r2 * 2048 + rowp, aC1 = aC0 ^ 64, bC = sb + 16384 + 4 * c2 * 1024 + lane * 16;
#define M4(acc, W, X, G0, G1, G2, G3) \
    acc[0] = MFMA(cat(W), X[0], acc[0]); SCHED(); G0; SCHED(); \
    acc[1] = MFMA(cat(W), X[1], acc[1]); SCHED(); G1; SCHED(); \
    acc[2] = MFMA(cat(W), X[2], acc[2]); SCHED(); G2; SCHED(); \
    acc[3] = MFMA(cat(W), X[3], acc[3]); SCHED(); G3; SCHED();
#define M2(acc, W, X, G0, G1) \
    acc[0] = MFMA(cat(W), X[0], acc[0]); SCHED(); G0; SCHED(); \
    acc[1] = MFMA(cat(W), X[1], acc[1]); SCHED(); G1; SCHED();
#define TIEF(f) TIE2(f.lo, f.hi)
#define KB(X, XN, XL, XH, KO, G0, G1, G2, G13, G14) \
    WL(15); TIE4(X[0], X[1], X[2], X[3]); TIEF(O0g); TIEF(O0u); SCHED(); \
    M4(gO0, O0g, X, G0, G1, G2, DTR(O0g.lo, wO0, KO)); \
    M4(uO0, O0u, X, DTR(O0g.hi, wO0, KO + 1024), DSR(XN[0], XL, 0), DSR(XN[1], XL, 2048), DTR(O0u.lo, wO0, KO + 16384)); \
    WL(15); TIEF(O1g); TIEF(O1u); TIEF(P0g); SCHED(); \
    M4(gO1, O1g, X, DTR(O0u.hi, wO0, KO + 17408), DSR(XN[2], XH, 0), DSR(XN[3], XH, 2048), DTR(O1g.lo, wO1, KO)); \
    M4(uO1, O1u, X, DTR(O1g.hi, wO1, KO + 1024), G13, G14, DTR(O1u.lo, wO1, KO + 16384)); \
    M2(gP0, P0g, X, DTR(O1u.hi, wO1, KO + 17408), DTR(P0g.lo, wP0, KO)); \
    WL(15); TIEF(P0u); TIEF(P1g); TIEF(P1u); TIEF(Sg); SCHED(); \
    M2(uP0, P0u, X, DTR(P0g.hi, wP0, KO + 1024), DTR(P0u.lo, wP0, KO + 16384)); \
    M2(gP1, P1g, X, DTR(P0u.hi, wP0, KO + 17408), DTR(P1g.lo, wP1, KO)); \
    M2(uP1, P1u, X, DTR(P1g.hi, wP1, KO + 1024), DTR(P1u.lo, wP1, KO + 16384)); \
    M2(gS, Sg, X, DTR(P1u.hi, wP1, KO + 17408), DTR(Sg.lo, wS, KO)); \
    WL(15); TIEF(Su); SCHED(); \
    M2(uS, Su, X, DTR(Sg.hi, wS, KO + 1024), DTR(Su.lo, wS, KO + 16384); DTR(Su.hi, wS, KO + 17408));
  {
    int reg = 1;
#pragma unroll 1
    for (int t = 0; t < 7; ++t) {
      const int dl = reg == 2 ? -2 * 49152 : 49152;
      reg = reg == 2 ? 0 : reg + 1;
      KB(xc, xn, xB10, xB11, 8192, ADV(xB00, dl), ADV(xB01, dl), , , );
      KB(xn, xc, xB00, xB01, 0, ADV(wO0, dl); ADV(wO1, dl), ADV(wP0, dl); ADV(wP1, dl), ADV(wS, dl); WL(0); BAR(), ADV(xB10, dl), ADV(xB11, dl));
      if (t == 1) {
        WAIT_V(2);
        if (lane == 0) __hip_atomic_store((GAS unsigned*)(ws + WS_FLAG) + (rb * 16 + cb) * 16 + w, 1u, __ATOMIC_RELAXED, __HIP_MEMORY_SCOPE_AGENT);
        SCHED();
      }
      if (t & 1) STAMP(4 + t);
    }
    const unsigned zr = sb + L_Z + (64 * r2 + rho) * 4;
    const unsigned zr0 = zr + ((s ^ (2 * c2)) * 64), zr1 = zr + ((s ^ (2 * c2) ^ 1) * 64), zr2 = zr + ((s ^ (2 * c2) ^ 2) * 64), zr3 = zr + ((s ^ (2 * c2) ^ 3) * 64);
    SCHED();
    KB(xc, xn, xB10, xB11, 8192, DSR32(cz[0], zr0, 0); DSR32(cz[1], zr1, 0), DSR32(cz[2], zr2, 0); DSR32(cz[3], zr3, 0), , , );
#define CZ(t) cz[t] = fminf(1.f + __builtin_amdgcn_exp2f(cz[t] * L2E), 3.0e38f)
    WL(15); TIE4(xn[0], xn[1], xn[2], xn[3]); TIEF(O0g); TIEF(O0u); SCHED();
    M4(gO0, O0g, xn, , , WL(0); TIE4(cz[0], cz[1], cz[2], cz[3]); TIEF(O1g); TIEF(O1u); TIEF(P0g); TIEF(P0u); TIEF(P1g); TIEF(P1u); TIEF(Sg); TIEF(Su); WAIT_V(0); BAR(), DSR(fa[0], aC0, 0));
    M4(uO0, O0u, xn, DSR(fb0[0], bC, 0), DSR(fb0[1], bC, 1024), DSR(fb0[2], bC, 2048), DSR(fa[1], aC0, 2048));
    M4(gO1, O1g, xn, DSR(fb0[3], bC, 3072), DSR(fa[2], aC0, 4096), DSR(fa[3], aC0, 6144), CZ(0));
    M4(uO1, O1u, xn, , CZ(1), , CZ(2));
    M2(gP0, P0g, xn, , CZ(3));
    M2(uP0, P0u, xn, , );
    M2(gP1, P1g, xn, , );
    M2(uP1, P1u, xn, , );
    M2(gS, Sg, xn, , );
    M2(uS, Su, xn, , );
  }
  STAMP(12);

  f32x4 acc[4][4] = {};
#define G4(i, B, G0, G1, G2, G3) \
    acc[i][0] = MFMA(B(0), fa[i], acc[i][0]); SCHED(); G0; SCHED(); \
    acc[i][1] = MFMA(B(1), fa[i], acc[i][1]); SCHED(); G1; SCHED(); \
    acc[i][2] = MFMA(B(2), fa[i], acc[i][2]); SCHED(); G2; SCHED(); \
    acc[i][3] = MFMA(B(3), fa[i], acc[i][3]); SCHED(); G3; SCHED();
#define FB0(e) fb0[e]
#define FB1(e) fb1[e]
#define KC(BC, bc, bn, AA, BO, SYNC, X9, X10, X12, E, e0) \
    WL(2); TIE4(bc[0], bc[1], bc[2], bc[3]); TIE2(fa[0], fa[1]); SCHED(); \
    G4(0, BC, E(e0 + 0), E(e0 + 1), E(e0 + 2), SYNC; DSR(fa[0], AA, 0); E(e0 + 3)); \
    G4(1, BC, DSR(bn[0], bC, BO); E(e0 + 4), DSR(bn[1], bC, BO + 1024); E(e0 + 5), DSR(bn[2], bC, BO + 2048); E(e0 + 6), DSR(fa[1], AA, 2048); E(e0 + 7)); \
    WL(6); TIE1(fa[2]); SCHED(); \
    G4(2, BC, DSR(bn[3], bC, BO + 3072); E(e0 + 8), X9; E(e0 + 9), X10; E(e0 + 10), DSR(fa[2], AA, 4096); E(e0 + 11)); \
    WL(7); TIE1(fa[3]); SCHED(); \
    G4(3, BC, X12; E(e0 + 12), E(e0 + 13), E(e0 + 14), DSR(fa[3], AA, 6144); E(e0 + 15));
#define STAGEC(E) { \
    const int dl = slot == 4 ? -4 * 32768 : 32768; \
    slot = slot == 4 ? 0 : slot + 1; \
    KC(FB0, fb0, fb1, aC1, 8192, , ADV(aC0, dl), ADV(bC, dl), , E, 0); \
    KC(FB1, fb1, fb0, aC0, 0, WL(0); BAR(), ADV(aC1, dl), , , E, 16); }
  int slot = 0;
  {
    const int kap = 7 * (j >> 1) + 4 * (j & 1);
    const __amdgpu_buffer_rsrc_t rh = mk_rsrc(ws + WS_HF + (size_t)(rb * 4 + s) * 114688, 114688);
    const int sg2 = s ^ (2 * c2);
    float t[8];
    u32x4 pc;
    auto ep = [&](auto K, const f32x4& ga, const f32x4& gb, const f32x4& ua, const f32x4& ub, float ca, float cb_, int soff, bool single) {
      constexpr int k = decltype(K)::v;
      constexpr int ord[32][2] = {{0, 1}, {2, 3}, {4, 5}, {6, -1}, {7, -1}, {8, -1}, {9, -1}, {10, -1}, {11, -1}, {12, 13}, {14, 15}, {16, -1}, {17, 18}, {19, 20}, {21, 22}, {23, -1},
                                  {24, 25}, {26, 27}, {28, 29}, {30, -1}, {31, -1}, {32, -1}, {33, -1}, {34, -1}, {35, -1}, {36, 37}, {38, 39}, {40, -1}, {41, 42}, {43, 44}, {45, -1}, {-1, -1}};
#pragma unroll
      for (int z = 0; z < 2; ++z) {
        constexpr int dummy = 0; (void)dummy;
        const int o = ord[k][z];
        if (o < 0) continue;
        if (o < 40) {
          const int grp = o / 20, r = o % 20, st = r / 4, v = grp * 4 + (r & 3);
          const float g = grp ? gb[r & 3] : ga[r & 3], u = grp ? ub[r & 3] : ua[r & 3], c = grp ? cb_ : ca;
          if (st == 0) t[v] = g * -L2E;
          else if (st == 1) t[v] = __builtin_amdgcn_exp2f(t[v]);
          else if (st == 2) t[v] = __builtin_fmaf(t[v], c, c);
          else if (st == 3) t[v] = __builtin_amdgcn_rcpf(t[v]);
          else t[v] = t[v] * u;
        } else if (o < 44) {
          pc[o - 40] = pk_bf16(t[2 * (o - 40)], t[2 * (o - 40) + 1]);
        } else if (o == 44) {
          if (single) {
            auto s0 = __builtin_amdgcn_permlane32_swap(pc[0], pc[2], false, false);
            auto s1 = __builtin_amdgcn_permlane32_swap(pc[1], pc[3], false, false);
            pc[0] = s0[0]; pc[2] = s0[1]; pc[1] = s1[0]; pc[3] = s1[1];
          }
        } else {
          const int vo = single ? ((sg2 ^ (q >> 1)) * 1024 + (((q & 1) + 2 * (j & 1)) * 16 + rho) * 16) : lane * 16;
          st_wt(pc, rh, vo, soff);
        }
      }
    };
#define EP_O(T) [&](auto K) { ep(K, gO0[T], gO1[T], uO0[T], uO1[T], cz[T], cz[T], (kap + c2) * 8192 + (4 * r2 + (sg2 ^ T)) * 1024, false); }
#define EP_P(T) [&](auto K) { ep(K, gP0[T], gP1[T], uP0[T], uP1[T], cz[T], cz[T], (kap + 2) * 8192 + (4 * r2 + (sg2 ^ T)) * 1024, false); }
#define EP_S() [&](auto K) { ep(K, gS[0], gS[1], uS[0], uS[1], cz[0], cz[1], (7 * (j >> 1) + 3) * 8192 + 4 * r2 * 1024, true); }
#define ECALL(k) e_(IC<k>{})
    { auto e_ = EP_O(0); STAGEC(ECALL); }
    { auto e_ = EP_O(1); STAGEC(ECALL); }
    { auto e_ = EP_O(2); STAGEC(ECALL); }
    { auto e_ = EP_O(3); STAGEC(ECALL); }
    STAMP(13);
    { auto e_ = EP_P(0); STAGEC(ECALL); }
    { auto e_ = EP_P(1); STAGEC(ECALL); }
    { auto e_ = EP_S(); STAGEC(ECALL); }
  }
  STAMP(14);
#define ENONE(k)
#pragma unroll 1
  for (int t = 7; t < 15; ++t) {
    STAGEC(ENONE);
    if (t == 7) {
      WAIT_V(0);
      if (lane == 0) __hip_atomic_store((GAS unsigned*)(ws + WS_FLAG) + (rb * 16 + cb) * 16 + 4 + w, 1u, __ATOMIC_RELAXED, __HIP_MEMORY_SCOPE_AGENT);
      SCHED();
    }
    if (t == 10) STAMP(15);
  }
  TF w0[4], w1[4];
  slot = slot == 4 ? 0 : slot + 1;
  unsigned hD = sb + slot * 32768 + 4 * r2 * 1024 + lane * 16;
  unsigned d000 = sb + slot * 32768 + 16384 + (8 * q + 4 * (q & 1) + r4) * 256 + (((8 * c2 + p) ^ swz) * 16) + (p & 1) * 8;
  unsigned d001 = d000 ^ 8, d010 = d000 ^ 64, d011 = d000 ^ 72, d100 = d000 ^ 1024, d101 = d000 ^ 1032, d110 = d000 ^ 1088, d111 = d000 ^ 1096;
#define RDW(wn, KO) DTR(wn[0].lo, d000, KO); DTR(wn[0].hi, d100, KO), DTR(wn[1].lo, d001, KO); DTR(wn[1].hi, d101, KO), \
                    DTR(wn[2].lo, d010, KO); DTR(wn[2].hi, d110, KO), DTR(wn[3].lo, d011, KO); DTR(wn[3].hi, d111, KO)
#define W0(e) cat(w0[e])
#define W1(e) cat(w1[e])
#define TIEW(wc) TIE4(wc[0].lo, wc[0].hi, wc[1].lo, wc[1].hi); TIE4(wc[2].lo, wc[2].hi, wc[3].lo, wc[3].hi)
#define KD_(BC, TIEB, KO, SYNC, R0, R1, R2, R3, X9, X10, X12, X13, X14) \
    WL(2); TIEB; TIE2(fa[0], fa[1]); SCHED(); \
    G4(0, BC, , , , SYNC; DSR(fa[0], hD, KO)); \
    G4(1, BC, R0, R1, R2, DSR(fa[1], hD, KO + 1024)); \
    WL(9); TIE1(fa[2]); SCHED(); \
    G4(2, BC, R3, X9, X10, DSR(fa[2], hD, KO + 2048)); \
    WL(11); TIE1(fa[3]); SCHED(); \
    G4(3, BC, X12, X13, X14, DSR(fa[3], hD, KO + 3072));
#define KD(...) KD_(__VA_ARGS__)
  KC(FB0, fb0, fb1, aC1, 8192, , , , , ENONE, 0);
  KD(FB1, TIE4(fb1[0], fb1[1], fb1[2], fb1[3]), 0, WL(0); BAR(), RDW(w0, 0), , , , , );
  STAMP(16);
  {
    float gt[4];
#pragma unroll
    for (int i = 0; i < 4; ++i) gt[i] = 1.f - __builtin_amdgcn_rcpf(c2 ? cz[i ^ 2] : cz[i]);
#pragma unroll
    for (int i = 0; i < 4; ++i)
#pragma unroll
      for (int e = 0; e < 4; ++e) acc[i][e] *= gt[i];
    SCHED();
  }
#pragma unroll 1
  for (int t = 0; t < 6; ++t) {
    const int dl = slot == 4 ? -4 * 32768 : 32768;
    slot = slot == 4 ? 0 : slot + 1;
    KD(W0, TIEW(w0), 8192, , RDW(w1, 8192), ADV(d000, dl); ADV(d100, dl), ADV(d001, dl); ADV(d101, dl), ADV(d010, dl); ADV(d110, dl), ADV(d011, dl); ADV(d111, dl), );
    ADV(hD, dl);
    KD(W1, TIEW(w1), 0, WL(0); BAR(), RDW(w0, 0), , , , , );
  }
  KD(W0, TIEW(w0), 8192, , RDW(w1, 8192), , , , , );
  WL(0); TIEW(w1); TIE4(fa[0], fa[1], fa[2], fa[3]); SCHED();
  G4(1, W1, , , , ); G4(2, W1, , , , ); G4(3, W1, , , , ); G4(0, W1, , , , );
  STAMP(17);

#pragma unroll
  for (int i = 1; i < 4; ++i)
#pragma unroll
    for (int h = 0; h < 2; ++h) {
      u32x4 v = {pk_bf16(acc[i][2 * h][0], acc[i][2 * h][1]), pk_bf16(acc[i][2 * h][2], acc[i][2 * h][3]),
                 pk_bf16(acc[i][2 * h + 1][0], acc[i][2 * h + 1][1]), pk_bf16(acc[i][2 * h + 1][2], acc[i][2 * h + 1][3])};
#pragma unroll
      for (int k = 0; k < 4; ++k) v[k] = min(v[k], 0xfffffffeu);
      st_wt(v, rp, lane * 16 + h * 1024, (((s ^ i) * 3 + (i - 1)) * 4 + w) * 2048);
    }
  STAMP(18);
  u32x4 pv[6];
  for (unsigned spins = 0; spins < (1u << 16); ++spins) {
#pragma unroll
    for (int k = 0; k < 6; ++k) pv[k] = __builtin_amdgcn_raw_buffer_load_b128(rp, lane * 16 + (k & 1) * 1024, ((s * 3 + (k >> 1)) * 4 + w) * 2048, 16);
    unsigned m = 0;
#pragma unroll
    for (int k = 0; k < 6; ++k) m = max(max(m, max(pv[k][0], pv[k][1])), max(pv[k][2], pv[k][3]));
    if (__all(m != 0xffffffffu)) break;
    asm volatile("" ::: "memory");
  }
  STAMP(19);
#pragma unroll
  for (int k = 0; k < 6; ++k)
#pragma unroll
    for (int d = 0; d < 4; ++d) {
      const int e = 2 * (k & 1) + (d >> 1), r = 2 * (d & 1);
      acc[0][e][r] += __builtin_bit_cast(float, pv[k][d] << 16);
      acc[0][e][r + 1] += __builtin_bit_cast(float, pv[k][d] & 0xffff0000u);
    }
  u16* op = out + (size_t)(rb * 128 + (4 * r2 + s) * 16 + rho) * D_ + nb * 128 + 64 * c2 + 8 * q;
#pragma unroll
  for (int h = 0; h < 2; ++h) {
    const unsigned a0 = pk_bf16(acc[0][2 * h][0], acc[0][2 * h][1]), a1 = pk_bf16(acc[0][2 * h][2], acc[0][2 * h][3]);
    const unsigned b0 = pk_bf16(acc[0][2 * h + 1][0], acc[0][2 * h + 1][1]), b1 = pk_bf16(acc[0][2 * h + 1][2], acc[0][2 * h + 1][3]);
    const bool o = q & 1;
    const u32x4 v = {o ? b0 : a0, o ? b1 : a1, o ? a0 : b0, o ? a1 : b1};
    *(u32x4*)(op + 32 * h) = v;
  }
  STAMP(20);
}

extern "C" void kernel_launch(void* const* d_in, const int* in_sizes, int n_in, void* d_out, int out_size, void* d_ws,
                              size_t ws_size, hipStream_t stream) {
  (void)hipMemsetAsync((char*)d_ws + WS_FLAG, 0, 16384, stream);
  fused<<<256, 512, 0, stream>>>((const u16*)d_in[0], (const u16*)d_in[1], (const u16*)d_in[2], (const u16*)d_in[3], (const u16*)d_in[4],
                                 (const u16*)d_in[6], (const u16*)d_in[7], (const u16*)d_in[8], (const u16*)d_in[9], (char*)d_ws, (u16*)d_out);
}
```

```cpp
#include <hip/hip_runtime.h>
#include <stdint.h>

#define LAS __attribute__((address_space(3)))
#define GAS __attribute__((address_space(1)))
#define DI __device__ __forceinline__
typedef unsigned short u16;
typedef __bf16 bf16x8 __attribute__((ext_vector_type(8)));
typedef __bf16 bf16x4 __attribute__((ext_vector_type(4)));
typedef __bf16 bf16x2 __attribute__((ext_vector_type(2)));
typedef float f32x4 __attribute__((ext_vector_type(4)));
typedef unsigned u32x4 __attribute__((ext_vector_type(4)));

constexpr int T_ = 2048, D_ = 512, F_ = 1792;
constexpr size_t WS_FLAG = 0;
constexpr size_t WS_ABF = 1u << 20;
constexpr size_t WS_HF = 8u << 20;
constexpr size_t WS_PART = 16u << 20;
constexpr int L_GW = 147456, L_Z = L_GW + 2048, L_TCH = L_GW + 4096, L_SIZE = 163840;

#define HAUX 16
#define INV() asm volatile("" ::: "memory")
#define STAG 1
#define TCH 2
#define EXP 0
#define MFMA(a, b, c) __builtin_amdgcn_mfma_f32_16x16x32_bf16((a), (b), (c), 0, 0, 0)
#define WAIT_V(n) asm volatile("s_waitcnt vmcnt(%0)" ::"n"(n) : "memory")
#define BAR() __builtin_amdgcn_s_barrier()
#define SCHED() __builtin_amdgcn_sched_barrier(0)
#define DSR(dst, addr, off) asm volatile("ds_read_b128 %0, %1 offset:%2" : "=v"(dst) : "v"(addr), "i"(off))
#define DSR32(dst, addr, off) asm volatile("ds_read_b32 %0, %1 offset:%2" : "=v"(dst) : "v"(addr), "i"(off))
#define DTR(dst, addr, off) asm volatile("ds_read_b64_tr_b16 %0, %1 offset:%2" : "=v"(dst) : "v"(addr), "i"(off))
#define ADV(x, d) asm volatile("v_add_u32 %0, %1, %0" : "+v"(x) : "s"(d))
#define WL(n) asm volatile("s_waitcnt lgkmcnt(%0)" ::"n"(n))
#define TIE1(a) asm volatile("" : "+v"(a))
#define TIE2(a, b) asm volatile("" : "+v"(a), "+v"(b))
#define TIE4(a, b, c, d) asm volatile("" : "+v"(a), "+v"(b), "+v"(c), "+v"(d))

#define STAMP(i)

struct TF { bf16x4 lo, hi; };
DI bf16x8 cat(const TF& f) { return __builtin_shufflevector(f.lo, f.hi, 0, 1, 2, 3, 4, 5, 6, 7); }
DI unsigned pk_bf16(float lo, float hi) { bf16x2 v; v[0] = (__bf16)lo; v[1] = (__bf16)hi; return __builtin_bit_cast(unsigned, v); }
DI __amdgpu_buffer_rsrc_t mk_rsrc(const void* p, unsigned bytes) { return __builtin_amdgcn_make_buffer_rsrc((void*)p, 0, bytes, 0x00020000); }
DI void glds(__amdgpu_buffer_rsrc_t r, LAS char* dst, int voff, int soff) {
  __builtin_amdgcn_raw_ptr_buffer_load_lds(r, (LAS void*)dst, 16, voff, soff, 0, 0);
}
DI void glds1(__amdgpu_buffer_rsrc_t r, LAS char* dst, int voff, int soff) {
  __builtin_amdgcn_raw_ptr_buffer_load_lds(r, (LAS void*)dst, 16, voff, soff, 0, HAUX);
}
DI void touch(__amdgpu_buffer_rsrc_t r, LAS char* dst, int voff) {
  __builtin_amdgcn_raw_ptr_buffer_load_lds(r, (LAS void*)dst, 4, voff, 0, 0, 0);
}
DI void st_wt(u32x4 v, __amdgpu_buffer_rsrc_t r, int voff, int soff) { __builtin_amdgcn_raw_buffer_store_b128(v, r, voff, soff, 16); }
template <int N> struct IC { static constexpr int v = N; };
template <int I, class T> DI const T& pick(const T& a0, const T& a1, const T& a2, const T& a3, const T& a4, const T& a5, const T& a6) {
  if constexpr (I == 0) return a0; else if constexpr (I == 1) return a1; else if constexpr (I == 2) return a2; else if constexpr (I == 3) return a3;
  else if constexpr (I == 4) return a4; else if constexpr (I == 5) return a5; else return a6;
}

__global__ __launch_bounds__(512, 2) void fused(const u16* __restrict__ x, const u16* __restrict__ Cr, const u16* __restrict__ Ci,
                                                const u16* __restrict__ Hr, const u16* __restrict__ Hi, const u16* __restrict__ Wg,
                                                const u16* __restrict__ Wu, const u16* __restrict__ Wd, const u16* __restrict__ gwp,
                                                char* __restrict__ ws, u16* __restrict__ out) {
  __shared__ __attribute__((aligned(1024))) char smem_[L_SIZE];
  LAS char* smem = (LAS char*)smem_;
  const int id = blockIdx.x, xcd = id & 7, idx = id >> 3;
  const int rb = (xcd & 1) * 8 + (idx >> 2), j = idx & 3, s = xcd >> 1, cb = 4 * s + j, nb = j;
  const int wid = __builtin_amdgcn_readfirstlane(threadIdx.x >> 6), lane = threadIdx.x & 63;
  const int off = STAG ? (rb & 7) : 0;
  STAMP(0);

  if (wid >= 4) {
    const int lw = wid - 4, hand = lw >> 1, l1 = lw & 1;
    const int r8 = lane >> 3, c8 = lane & 7, row4 = lane >> 4, ph = lane & 15;
    const int sw0 = (c8 ^ (r8 >> 1)) * 16, sw1 = sw0 ^ 64;
    const __amdgpu_buffer_rsrc_t rx = mk_rsrc(x + (size_t)rb * 128 * D_, 128 * 1024);
    {
      const __amdgpu_buffer_rsrc_t rh = mk_rsrc((cb < 8 ? Hr : Hi) + (size_t)(cb & 7) * 64 * D_, 64 * 1024);
      const int rx_ = (8 * (r8 >> 2) + (r8 & 3)) * 1024;
      const int rh_ = (32 * (lw >> 1) + 8 * (r8 >> 2) + 4 * (l1 ^ (r8 >> 2)) + (r8 & 3)) * 1024;
      const int vxa[2] = {rx_ + sw0, rx_ + sw1}, vha[2] = {rh_ + sw0, rh_ + 16384 + sw1};
      auto issueA = [&](int t) {
        LAS char* sb = smem + (t % 6) * 24576;
        const int ko = ((t + off) & 7) * 128;
#pragma unroll
        for (int i = 0; i < 4; ++i) glds(rx, sb + (4 * lw + i) * 1024, vxa[i & 1], (32 * lw + 16 * (i & 1) + 4 * (i >> 1)) * 1024 + ko);
#pragma unroll
        for (int i = 0; i < 2; ++i) glds(rh, sb + 16384 + (2 * lw + i) * 1024, vha[i], ko);
      };
      if (lw == 0) glds(mk_rsrc(gwp, 1024), smem + L_GW, lane * 16, 0);
      issueA(0); issueA(1); issueA(2);
      WAIT_V(12); STAMP(1); BAR();
      issueA(3); issueA(4); issueA(5);
      WAIT_V(24); BAR(); issueA(6);
      WAIT_V(24); BAR(); issueA(7);
    }
    const int tbx0 = (2 * lw & 4) | ((2 * lw & 3) ^ s), tbx1 = tbx0 ^ 1;
    {
      const __amdgpu_buffer_rsrc_t rw = mk_rsrc(hand ? Wu : Wg, D_ * F_ * 2);
      const int vxb[2] = {r8 * 1024 + sw0, r8 * 1024 + 8192 + sw1};
      int vw[2];
#pragma unroll
      for (int hb = 0; hb < 2; ++hb) {
        const int v = ph ^ ((row4 & 1) | ((row4 >> 1) << 2) | (hb << 3));
        int gu = v + 2 * (cb & 1); gu = gu >= 14 ? gu - 14 : gu;
        vw[hb] = v < 14 ? row4 * (F_ * 2) + cb * 224 + gu * 16 : (int)0x80000000;
      }
      auto issueB = [&](int t, int reg) {
        LAS char* sb = smem + reg * 49152;
        t = (t + off) & 7;
#pragma unroll
        for (int i = 0; i < 4; ++i) glds(rx, sb + (4 * lw + i) * 1024, vxb[i & 1], ((i >> 1) ? tbx1 : tbx0) * 16384 + t * 128);
#pragma unroll
        for (int i = 0; i < 8; ++i) glds(rw, sb + 16384 + (8 * lw + i) * 1024, vw[(i >> 1) & 1], (64 * t + 32 * l1 + 4 * i) * (F_ * 2));
      };
      WAIT_V(24); BAR();
      WAIT_V(18); BAR(); issueB(0, 1);
      WAIT_V(24); BAR();
      WAIT_V(18); BAR(); issueB(1, 2);
      WAIT_V(24); BAR();
      int reg = 0;
#pragma unroll 1
      for (int t = 2; t < 7; ++t) {
        WAIT_V(12); BAR(); issueB(t, reg);
        reg = reg == 2 ? 0 : reg + 1;
      }
      WAIT_V(12); BAR();
      unsigned pv = 1u;
      const int fa = (((4 * s + ((lane >> 2) & 3)) * 16 + 2 * nb + ((lane >> 4) & 1) + 8 * (lane >> 5)) * 16 + (lane & 3)) * 4;
      if (hand) asm volatile("global_load_dword %0, %1, %2 sc1" : "=v"(pv) : "v"(fa), "s"(ws) : "memory");
      issueB(7, 2);
      asm volatile("s_waitcnt vmcnt(12)" : "+v"(pv)::"memory"); BAR();
      if (hand) {
        STAMP(2);
        if (!__all(pv != 0u)) {
          for (unsigned spins = 0; spins < (1u << 16); ++spins) {
            if (__all(__hip_atomic_load((GAS unsigned*)(ws + fa), __ATOMIC_RELAXED, __HIP_MEMORY_SCOPE_AGENT) != 0u)) break;
            __builtin_amdgcn_s_sleep(1);
          }
        }
        INV();
        STAMP(3);
      }
    }
    int slot = 0;
    {
      const size_t coff = (size_t)rb * 128 * T_ + s * 512;
      const char* abf = ws + WS_ABF + (size_t)(s * 4 + nb) * 262144;
      const unsigned n0 = hand ? 262144u : (unsigned)(((size_t)T_ * T_ - coff) * 2), n1 = hand ? 131072u : n0;
      const __amdgpu_buffer_rsrc_t r0 = mk_rsrc(hand ? abf : (const char*)(Cr + coff), n0);
      const __amdgpu_buffer_rsrc_t r1 = mk_rsrc(hand ? abf + 131072 : (const char*)(Ci + coff), n1);
      const int vo[2] = {hand ? lane * 16 : r8 * (T_ * 2) + sw0, hand ? lane * 16 : r8 * (T_ * 2) + sw1};
      const int inc = hand ? 16384 : 128;
      int ro[8];
#pragma unroll
      for (int i = 0; i < 8; ++i) {
        const int P = 4 * l1 + (i >> 1), tb = (P & 4) | ((P & 3) ^ s);
        ro[i] = hand ? (8 * l1 + i) * 1024 : (16 * tb + 8 * (i & 1)) * (T_ * 2);
      }
      auto issueC = [&](int t) {
        LAS char* sb = smem + slot * 32768 + lw * 8192;
        const __amdgpu_buffer_rsrc_t r = t < 8 ? r0 : r1;
        if (hand) {
#pragma unroll
          for (int i = 0; i < 8; ++i) glds1(r, sb + i * 1024, vo[i & 1], ro[i] + ((t + off) & 7) * inc);
        } else {
#pragma unroll
          for (int i = 0; i < 8; ++i) glds(r, sb + i * 1024, vo[i & 1], ro[i] + ((t + off) & 7) * inc);
        }
        slot = slot == 4 ? 0 : slot + 1;
      };
      issueC(0);
      WAIT_V(8); BAR(); issueC(1); issueC(2);
      WAIT_V(16); BAR(); issueC(3); issueC(4);
#pragma unroll 1
      for (int t = 5; t < 15; ++t) { WAIT_V(24); BAR(); issueC(t); }
      WAIT_V(24); BAR();
      unsigned pv = 1u;
      const int pl = lane < 28 ? lane : 0;
      const int fb = (pl < 16 ? (rb * 16 + 4 * s + (pl >> 2)) * 16 + 4 + (pl & 3) : (rb * 16 + 4 * (s ^ ((pl >> 2) - 3)) + j) * 16 + 4 + (pl & 3)) * 4;
      if (hand) asm volatile("global_load_dword %0, %1, %2 sc1" : "=v"(pv) : "v"(fb), "s"(ws) : "memory");
      issueC(15);
      WAIT_V(24); BAR();
      if (hand) {
        asm volatile("s_waitcnt vmcnt(8)" : "+v"(pv)::"memory");
        STAMP(4);
        if (!__all(pv != 0u)) {
          for (unsigned spins = 0; spins < (1u << 16); ++spins) {
            if (__all(__hip_atomic_load((GAS unsigned*)(ws + fb), __ATOMIC_RELAXED, __HIP_MEMORY_SCOPE_AGENT) != 0u)) break;
            __builtin_amdgcn_s_sleep(1);
          }
        }
        INV();
        STAMP(5);
      }
    }
    {
      const size_t woff = (size_t)448 * s * D_ + nb * 128;
      const __amdgpu_buffer_rsrc_t r = mk_rsrc(hand ? ws + WS_HF + (size_t)(rb * 4 + s) * 114688 : (const char*)(Wd + woff),
                                               hand ? 114688u : (unsigned)(((size_t)F_ * D_ - woff) * 2));
      const int u0 = ph ^ ((row4 & 1) | ((row4 >> 1) << 2));
      const int vo[2] = {hand ? lane * 16 : row4 * 1024 + u0 * 16, hand ? lane * 16 : row4 * 1024 + (u0 ^ 8) * 16};
      const int inc = hand ? 16384 : 65536;
      int ro[8];
#pragma unroll
      for (int i = 0; i < 8; ++i) ro[i] = hand ? l1 * 8192 + ((i & 4) | ((i & 3) ^ s)) * 1024 : (32 * l1 + 4 * i) * 1024;
      auto issueD = [&](int t) {
        LAS char* sb = smem + slot * 32768 + (lw ^ 2) * 8192;
        if (hand) {
#pragma unroll
          for (int i = 0; i < 8; ++i) glds1(r, sb + i * 1024, vo[(i >> 1) & 1], ro[i] + t * inc);
        } else {
#pragma unroll
          for (int i = 0; i < 8; ++i) glds(r, sb + i * 1024, vo[(i >> 1) & 1], ro[i] + t * inc);
        }
        slot = slot == 4 ? 0 : slot + 1;
      };
      issueD(0);
#pragma unroll 1
      for (int t = 1; t < 7; ++t) { WAIT_V(24); BAR(); issueD(t); }
      WAIT_V(24); BAR();
      WAIT_V(16); BAR();
      WAIT_V(8); BAR();
      WAIT_V(0); STAMP(6); BAR();
    }
    return;
  }

  const int w = wid, r2 = w >> 1, c2 = w & 1;
  const int rho = lane & 15, q = lane >> 4, r4 = rho >> 2, p = lane & 3;
  const unsigned sb = (unsigned)(size_t)smem;
  const float L2E = 1.4426950408889634f;
  const int tile = rb * 4 + nb;
  const __amdgpu_buffer_rsrc_t rp = mk_rsrc(ws + WS_PART + (size_t)tile * 98304, 98304);
  const __amdgpu_buffer_rsrc_t rcr = mk_rsrc(Cr + (size_t)rb * 128 * T_ + s * 512, 127 * T_ * 2 + 1024);
  const int vtc = (32 * j + (lane & 31)) * (T_ * 2) + (2 * w + (lane >> 5)) * 128;
  if (TCH & 1) {
    touch(mk_rsrc(x + (size_t)rb * 128 * D_, 128 * 1024), smem + L_TCH, (32 * j + 8 * w + (lane >> 3)) * 1024 + (lane & 7) * 128);
    const int vt = w == 0 ? (8 * (rb & 7) + (lane >> 3)) * 1024 + (lane & 7) * 128 : (64 * (rb & 7) + lane) * (F_ * 2) + cb * 224 + (w == 1 ? 0 : w == 2 ? 112 : 220);
    const u16* hp = (cb < 8 ? Hr : Hi) + (size_t)(cb & 7) * 64 * D_;
    const unsigned nbt = w == 0 ? 65536u : (unsigned)(D_ * F_ * 2);
    touch(mk_rsrc(w == 0 ? hp : Wg, nbt), smem + L_TCH, vt);
    touch(mk_rsrc(w == 0 ? hp : Wu, nbt), smem + L_TCH, vt);
  }
  const unsigned rowp = rho * 128 + ((q ^ (rho >> 1)) * 16);
  const unsigned swz = (r4 & 1) | ((r4 >> 1) << 2) | ((q & 1) << 3);

  unsigned xB00 = sb + 49152 + (4 * r2 + 2 * c2) * 2048 + rowp, xB01 = xB00 ^ 4096, xB10 = xB00 ^ 64, xB11 = xB01 ^ 64;
  const unsigned wrow = sb + 49152 + 16384 + (8 * q + r4) * 256;
  unsigned wO0 = wrow + (((4 * c2 + p) ^ swz) * 16) + (p & 1) * 8, wO1 = wO0 ^ 8;
  unsigned wP0 = wrow + (((8 + p) ^ swz) * 16) + (p & 1) * 8, wP1 = wP0 ^ 8;
  const unsigned gam = (0x2130u >> (4 * p)) & 3u;
  unsigned wS = wrow + (((12 + (gam >> 1)) ^ swz) * 16) + (gam & 1) * 8;
  bf16x8 xc[4], xn[4];
  TF O0g, O0u, O1g, O1u, P0g, P0u, P1g, P1u, Sg0, Su0, Sg1, Su1;
  f32x4 gO0[4] = {}, uO0[4] = {}, gO1[4] = {}, uO1[4] = {}, gP0[2] = {}, uP0[2] = {}, gP1[2] = {}, uP1[2] = {}, gS[2] = {}, uS[2] = {};
  TIE4(gO0[0], gO0[1], gO0[2], gO0[3]); TIE4(uO0[0], uO0[1], uO0[2], uO0[3]); TIE4(gO1[0], gO1[1], gO1[2], gO1[3]); TIE4(uO1[0], uO1[1], uO1[2], uO1[3]);
  TIE4(gP0[0], gP0[1], uP0[0], uP0[1]); TIE4(gP1[0], gP1[1], uP1[0], uP1[1]); TIE4(gS[0], gS[1], uS[0], uS[1]);

  {
    struct FA { bf16x8 x0, x1, h0, h1, h2, h3, g; };
    FA f0, f1;
    unsigned xA0 = sb + 4096 * w + rowp, xA1 = xA0 ^ 64, hA0 = sb + 16384 + rowp, hA1 = hA0 ^ 64, gA = sb + L_GW + q * 16 + off * 128;
    f32x4 ac[2][4] = {}, z0 = {}, z1 = {};
    TIE4(ac[0][0], ac[0][1], ac[0][2], ac[0][3]); TIE4(ac[1][0], ac[1][1], ac[1][2], ac[1][3]); TIE2(z0, z1);
#define KA_(fc, G0, G1, G2, G3, G4, G5, G6, G7, G8) \
    ac[0][0] = MFMA(fc.x0, fc.h0, ac[0][0]); SCHED(); G0; SCHED(); \
    ac[1][0] = MFMA(fc.x1, fc.h0, ac[1][0]); SCHED(); G1; SCHED(); \
    ac[0][1] = MFMA(fc.x0, fc.h1, ac[0][1]); SCHED(); G2; SCHED(); \
    ac[1][1] = MFMA(fc.x1, fc.h1, ac[1][1]); SCHED(); G3; SCHED(); \
    ac[0][2] = MFMA(fc.x0, fc.h2, ac[0][2]); SCHED(); G4; SCHED(); \
    ac[1][2] = MFMA(fc.x1, fc.h2, ac[1][2]); SCHED(); G5; SCHED(); \
    ac[0][3] = MFMA(fc.x0, fc.h3, ac[0][3]); SCHED(); G6; SCHED(); \
    ac[1][3] = MFMA(fc.x1, fc.h3, ac[1][3]); SCHED(); G7; SCHED(); \
    z0 = MFMA(fc.x0, fc.g, z0); SCHED(); G8; SCHED(); \
    z1 = MFMA(fc.x1, fc.g, z1); SCHED();
#define KA(...) KA_(__VA_ARGS__)
#define KA0(X7, X8) \
    TIE4(f0.x0, f0.x1, f0.h0, f0.h1); WL(3); SCHED(); \
    KA(f0, DSR(f1.x0, xA1, 0), DSR(f1.h0, hA1, 0), DSR(f1.x1, xA1, 2048), DSR(f1.h1, hA1, 2048); TIE2(f0.h2, f0.h3); TIE1(f0.g); WL(4), \
       DSR(f1.h2, hA1, 4096), DSR(f1.h3, hA1, 6144), DSR(f1.g, gA, 64), X7, X8)
#define KA1_HEAD TIE4(f1.x0, f1.x1, f1.h0, f1.h1); WL(3); SCHED();
#define KA1_SYNC TIE2(f1.h2, f1.h3); TIE1(f1.g); WL(0); BAR()
    BAR();
    STAMP(1);
    if (TCH & 2) touch(rcr, smem + L_TCH, vtc);
    DSR(f0.x0, xA0, 0); DSR(f0.h0, hA0, 0); DSR(f0.x1, xA0, 2048); DSR(f0.h1, hA0, 2048); DSR(f0.h2, hA0, 4096); DSR(f0.h3, hA0, 6144); DSR(f0.g, gA, 0);
#pragma unroll 1
    for (int t = 0; t < 7; ++t) {
      const int dl = t == 5 ? -5 * 24576 : 24576, dg = ((t + off) & 7) == 7 ? -896 : 128;
      KA0(ADV(xA0, dl); ADV(hA0, dl), ADV(gA, dg));
      KA1_HEAD;
      KA(f1, ADV(xA1, dl); ADV(hA1, dl), KA1_SYNC; DSR(f0.x0, xA0, 0), DSR(f0.h0, hA0, 0), DSR(f0.x1, xA0, 2048), DSR(f0.h1, hA0, 2048),
         DSR(f0.h2, hA0, 4096), DSR(f0.h3, hA0, 6144), DSR(f0.g, gA, 0), );
    }
    KA0(, );
    KA1_HEAD;
    KA(f1, , KA1_SYNC; DSR(xc[0], xB00, 0); DSR(xc[1], xB00, 2048),
           DTR(O0g.lo, wO0, 0); DTR(O0g.hi, wO0, 1024); DSR(xc[2], xB01, 0),
           DSR(xc[3], xB01, 2048); DTR(O0u.lo, wO0, 16384); DTR(O0u.hi, wO0, 17408),
           DTR(Sg0.lo, wS, 0); DTR(Sg0.hi, wS, 1024); DTR(O1g.lo, wO1, 0),
           DTR(O1g.hi, wO1, 1024); DTR(Su0.lo, wS, 16384); DTR(Su0.hi, wS, 17408),
           DTR(O1u.lo, wO1, 16384); DTR(O1u.hi, wO1, 17408); DTR(P0g.lo, wP0, 0),
           DTR(P0g.hi, wP0, 1024); DTR(P0u.lo, wP0, 16384); DTR(P0u.hi, wP0, 17408),
           DTR(P1g.lo, wP1, 0); DTR(P1g.hi, wP1, 1024); DTR(P1u.lo, wP1, 16384); DTR(P1u.hi, wP1, 17408));
    STAMP(2);
    const unsigned sg = cb < 8 ? 0u : 0x80008000u;
    const __amdgpu_buffer_rsrc_t ra = mk_rsrc(ws + WS_ABF + (size_t)(((rb >> 2) * 4 + ((cb & 7) >> 1)) * 16 + (cb & 8) + 2 * (rb & 3) + r2) * 16384 + (c2 * 8 + 4 * (cb & 1)) * 1024, 4096);
#pragma unroll
    for (int e = 0; e < 4; ++e) {
      u32x4 v = {pk_bf16(ac[0][e][0], ac[0][e][1]) ^ sg, pk_bf16(ac[0][e][2], ac[0][e][3]) ^ sg, pk_bf16(ac[1][e][0], ac[1][e][1]) ^ sg, pk_bf16(ac[1][e][2], ac[1][e][3]) ^ sg};
      st_wt(v, ra, lane * 16, e * 1024);
    }
    if (TCH & 2) {
      touch(mk_rsrc(Ci + (size_t)rb * 128 * T_ + s * 512, 127 * T_ * 2 + 1024), smem + L_TCH, vtc);
      touch(mk_rsrc(Wd + (size_t)448 * s * D_ + nb * 128, 447 * 1024 + 256), smem + L_TCH, (56 * (rb & 7) + (lane < 56 ? lane : 55)) * 1024 + c2 * 128);
    }
    const unsigned zw = sb + L_Z + (32 * w + 8 * q + 4 * (rho & 1)) * 4;
    const f32x4 zs = (rho & 1) ? z1 : z0;
    if (rho < 2) asm volatile("ds_write_b128 %0, %1" ::"v"(zw), "v"(zs));
    SCHED();
    STAMP(3);
  }

  float cz[4];
  bf16x8 fa[3], fz0, fz1, fb0[4], fb1[4];
  unsigned aC0 = sb + 4 * r2 * 2048 + rowp, aC1 = aC0 ^ 64, bC = sb + 16384 + 4 * c2 * 1024 + lane * 16;
#define M4(acc, W, X, G0, G1, G2, G3) \
    acc[0] = MFMA(cat(W), X[0], acc[0]); SCHED(); G0; SCHED(); \
    acc[1] = MFMA(cat(W), X[1], acc[1]); SCHED(); G1; SCHED(); \
    acc[2] = MFMA(cat(W), X[2], acc[2]); SCHED(); G2; SCHED(); \
    acc[3] = MFMA(cat(W), X[3], acc[3]); SCHED(); G3; SCHED();
#define M2(acc, W, X, G0, G1) \
    acc[0] = MFMA(cat(W), X[0], acc[0]); SCHED(); G0; SCHED(); \
    acc[1] = MFMA(cat(W), X[1], acc[1]); SCHED(); G1; SCHED();
#define TIEF(f) TIE2(f.lo, f.hi)
#define KB(X, XN, SG, SU, SGN, SUN, XL, XH, KO, G0, G1, G2, G27, Z, NW8) \
    TIE4(X[0], X[1], X[2], X[3]); TIEF(O0g); TIEF(O0u); WL(15); SCHED(); \
    M4(gO0, O0g, X, G0, G1, G2, DSR(XN[0], XL, 0)); \
    M4(uO0, O0u, X, DSR(XN[1], XL, 2048), DTR(O0g.lo, wO0, KO), DTR(O0g.hi, wO0, KO + 1024), DSR(XN[2], XH, 0); TIEF(O1g); TIEF(O1u); Z(-1); WL(NW8)); \
    M4(gO1, O1g, X, DSR(XN[3], XH, 2048); Z(0), DTR(O0u.lo, wO0, KO + 16384); Z(1), DTR(O0u.hi, wO0, KO + 17408); Z(2), DTR(SGN.lo, wS, KO); Z(3)); \
    M4(uO1, O1u, X, DTR(SGN.hi, wS, KO + 1024); Z(4), DTR(O1g.lo, wO1, KO); Z(5), DTR(O1g.hi, wO1, KO + 1024); Z(6), DTR(SUN.lo, wS, KO + 16384); Z(7); TIEF(P0g); TIEF(P0u); WL(15)); \
    M2(gP0, P0g, X, DTR(SUN.hi, wS, KO + 17408); Z(8), DTR(O1u.lo, wO1, KO + 16384); Z(9)); \
    M2(uP0, P0u, X, DTR(O1u.hi, wO1, KO + 17408); Z(10), DTR(P0g.lo, wP0, KO); Z(11); TIEF(P1g); TIEF(P1u); TIEF(SG); TIEF(SU); WL(15)); \
    M2(gP1, P1g, X, DTR(P0g.hi, wP0, KO + 1024); Z(12), DTR(P0u.lo, wP0, KO + 16384); Z(13)); \
    M2(uP1, P1u, X, DTR(P0u.hi, wP0, KO + 17408); Z(14), DTR(P1g.lo, wP1, KO); Z(15)); \
    M2(gS, SG, X, DTR(P1g.hi, wP1, KO + 1024), DTR(P1u.lo, wP1, KO + 16384)); \
    M2(uS, SU, X, DTR(P1u.hi, wP1, KO + 17408), G27);
#define ZN(k)
  const int kap = 7 * (j >> 1) + 4 * (j & 1), sg2 = s ^ (2 * c2);
  const __amdgpu_buffer_rsrc_t rh = mk_rsrc(ws + WS_HF + (size_t)(rb * 4 + s) * 114688, 114688);
  float et[8];
  u32x4 pc;
  auto eop = [&](auto N) {
    constexpr int n = decltype(N)::v, pi = n / 46, o = n % 46;
    if constexpr (pi < 7) {
      if constexpr (o < 40) {
        constexpr int grp = o / 20, r = o % 20, st = r / 4, e = r & 3, v = grp * 4 + e;
        const f32x4& g = grp ? pick<pi>(gO1[0], gO1[1], gO1[2], gO1[3], gP1[0], gP1[1], gS[1]) : pick<pi>(gO0[0], gO0[1], gO0[2], gO0[3], gP0[0], gP0[1], gS[0]);
        const f32x4& u = grp ? pick<pi>(uO1[0], uO1[1], uO1[2], uO1[3], uP1[0], uP1[1], uS[1]) : pick<pi>(uO0[0], uO0[1], uO0[2], uO0[3], uP0[0], uP0[1], uS[0]);
        const float c = cz[pi < 4 ? pi : pi < 6 ? pi - 4 : grp];
        if constexpr (st == 0) et[v] = g[e] * -L2E;
        else if constexpr (st == 1) et[v] = __builtin_amdgcn_exp2f(et[v]);
        else if constexpr (st == 2) et[v] = __builtin_fmaf(et[v], c, c);
        else if constexpr (st == 3) et[v] = __builtin_amdgcn_rcpf(et[v]);
        else et[v] = et[v] * u[e];
      } else if constexpr (o < 44) {
        pc[o - 40] = pk_bf16(et[2 * (o - 40)], et[2 * (o - 40) + 1]);
      } else if constexpr (o == 44) {
        if constexpr (pi == 6) {
          auto s0 = __builtin_amdgcn_permlane32_swap(pc[0], pc[2], false, false);
          auto s1 = __builtin_amdgcn_permlane32_swap(pc[1], pc[3], false, false);
          pc[0] = s0[0]; pc[2] = s0[1]; pc[1] = s1[0]; pc[3] = s1[1];
        }
      } else {
        if constexpr (pi == 6) st_wt(pc, rh, (sg2 ^ (q >> 1)) * 1024 + (((q & 1) + 2 * (j & 1)) * 16 + rho) * 16, (7 * (j >> 1) + 3) * 8192 + 4 * r2 * 1024);
        else st_wt(pc, rh, lane * 16, (kap + (pi < 4 ? c2 : 2)) * 8192 + (4 * r2 + (sg2 ^ (pi < 4 ? pi : pi - 4))) * 1024);
      }
    }
  };
#define E1(n) eop(IC<(n)>{})
#define E2(n) eop(IC<(n)>{}); eop(IC<(n) + 1>{})
  {
    int reg = 1;
    const u32x4 empty = {0xffffffffu, 0xffffffffu, 0xffffffffu, 0xffffffffu};
#pragma unroll 1
    for (int t = 0; t < 7; ++t) {
      const int dl = reg == 2 ? -2 * 49152 : 49152;
      reg = reg == 2 ? 0 : reg + 1;
      KB(xc, xn, Sg0, Su0, Sg1, Su1, xB10, xB11, 8192, ADV(xB00, dl), ADV(xB01, dl), , ADV(wO0, dl); ADV(wO1, dl), ZN, 13);
      KB(xn, xc, Sg1, Su1, Sg0, Su0, xB00, xB01, 0, ADV(wP0, dl); ADV(wP1, dl), ADV(wS, dl); ADV(xB10, dl), ADV(xB11, dl); WL(0); BAR(), , ZN, 13);
      if (t == 1) {
        WAIT_V((TCH & 2) ? 3 : 1);
        if (lane == 0) __hip_atomic_store((GAS unsigned*)(ws + WS_FLAG) + (rb * 16 + cb) * 16 + w, 1u, __ATOMIC_RELAXED, __HIP_MEMORY_SCOPE_AGENT);
      }
      const int ti = t < 5 ? t : 5;
      st_wt(empty, rp, lane * 16, ((s * 3 + (ti >> 1)) * 4 + w) * 2048 + (ti & 1) * 1024);
      SCHED();
      if (t & 1) STAMP(4 + t);
    }
    const unsigned zr = sb + L_Z + (64 * r2 + rho) * 4;
    const unsigned zr0 = zr + (sg2 * 64), zr1 = zr + ((sg2 ^ 1) * 64), zr2 = zr + ((sg2 ^ 2) * 64), zr3 = zr + ((sg2 ^ 3) * 64);
    auto zop = [&](auto N) {
      constexpr int n = decltype(N)::v, st = n >> 2, v = n & 3;
      if constexpr (n == -1) { TIE4(cz[0], cz[1], cz[2], cz[3]); }
      else if constexpr (st == 0) cz[v] *= L2E;
      else if constexpr (st == 1) cz[v] = __builtin_amdgcn_exp2f(cz[v]);
      else if constexpr (st == 2) cz[v] += 1.f;
      else cz[v] = fminf(cz[v], 3.0e38f);
    };
    SCHED();
#define ZQ(k) zop(IC<k>{})
    KB(xc, xn, Sg0, Su0, Sg1, Su1, xB10, xB11, 8192, DSR32(cz[0], zr0, 0); DSR32(cz[1], zr1, 0), DSR32(cz[2], zr2, 0); DSR32(cz[3], zr3, 0), , , ZQ, 5);
    TIE4(xn[0], xn[1], xn[2], xn[3]); TIEF(O0g); TIEF(O0u); WL(15); SCHED();
    M4(gO0, O0g, xn, , , TIEF(O1g); TIEF(O1u); TIEF(P0g); TIEF(P0u); TIEF(P1g); TIEF(P1u); TIEF(Sg1); TIEF(Su1); WL(0); WAIT_V(0); BAR(), DSR(fb0[0], bC, 0));
    M4(uO0, O0u, xn, DSR(fb0[1], bC, 1024), DSR(fa[0], aC0, 0), DSR(fb0[2], bC, 2048), DSR(fb0[3], bC, 3072));
    M4(gO1, O1g, xn, DSR(fz0, aC0, 6144), DSR(fa[1], aC0, 2048), DSR(fa[2], aC0, 4096), E2(0));
    M4(uO1, O1u, xn, E2(2), E2(4), E2(6), E2(8));
    M2(gP0, P0g, xn, E2(10), E2(12));
    M2(uP0, P0u, xn, E2(14), E2(16));
    M2(gP1, P1g, xn, E2(18), E2(20));
    M2(uP1, P1u, xn, E2(22), E2(24));
    M2(gS, Sg1, xn, E2(26), E2(28));
    M2(uS, Su1, xn, E2(30), E2(32));
  }
  STAMP(12);

  f32x4 acc[4][4] = {};
#define G4(i, A, B, G0, G1, G2, G3) \
    acc[i][0] = MFMA(B(0), A, acc[i][0]); SCHED(); G0; SCHED(); \
    acc[i][1] = MFMA(B(1), A, acc[i][1]); SCHED(); G1; SCHED(); \
    acc[i][2] = MFMA(B(2), A, acc[i][2]); SCHED(); G2; SCHED(); \
    acc[i][3] = MFMA(B(3), A, acc[i][3]); SCHED(); G3; SCHED();
#define FB0(e) fb0[e]
#define FB1(e) fb1[e]
#define KC(BC, bc, bn, zc, zn, AA, BO, SYNC, X10, X11, e1, e2, e0) \
    TIE4(bc[0], bc[1], bc[2], bc[3]); TIE2(fa[0], fa[1]); TIE1(zc); WL(1); SCHED(); \
    G4(0, fa[0], BC, e2(e0 + 0), e2(e0 + 2), SYNC; e2(e0 + 4), DSR(bn[0], bC, BO); e1(e0 + 6)); \
    G4(1, fa[1], BC, DSR(bn[1], bC, BO + 1024); e1(e0 + 7), DSR(fa[0], AA, 0); e1(e0 + 8), DSR(bn[2], bC, BO + 2048); e1(e0 + 9), DSR(bn[3], bC, BO + 3072); e1(e0 + 10); TIE1(fa[2]); WL(5)); \
    G4(2, fa[2], BC, DSR(zn, AA, 6144); e1(e0 + 11), DSR(fa[1], AA, 2048); e1(e0 + 12), X10; e2(e0 + 13), X11; e2(e0 + 15)); \
    G4(3, zc, BC, e2(e0 + 17), DSR(fa[2], AA, 4096); e1(e0 + 19), e2(e0 + 20), e2(e0 + 22));
#define STAGEC(e1, e2, e0) { \
    const int dl = slot == 4 ? -4 * 32768 : 32768; \
    slot = slot == 4 ? 0 : slot + 1; \
    KC(FB0, fb0, fb1, fz0, fz1, aC1, 8192, , ADV(aC0, dl), ADV(bC, dl), e1, e2, e0); \
    KC(FB1, fb1, fb0, fz1, fz0, aC0, 0, WL(0); BAR(), ADV(aC1, dl), , e1, e2, e0 + 24); }
  int slot = 0;
  STAGEC(E1, E2, 34); STAGEC(E1, E2, 82); STAGEC(E1, E2, 130);
  STAMP(13);
  STAGEC(E1, E2, 178); STAGEC(E1, E2, 226); STAGEC(E1, E2, 274);
  STAMP(14);
#define EN(k)
#pragma unroll 1
  for (int t = 6; t < 15; ++t) {
    STAGEC(EN, EN, 0);
    if (t == 6) {
      WAIT_V(0);
      if (lane == 0) __hip_atomic_store((GAS unsigned*)(ws + WS_FLAG) + (rb * 16 + cb) * 16 + 4 + w, 1u, __ATOMIC_RELAXED, __HIP_MEMORY_SCOPE_AGENT);
      SCHED();
    }
    if (t == 10) STAMP(15);
  }
  TF w0[4], w1[4];
  slot = slot == 4 ? 0 : slot + 1;
  unsigned hD = sb + slot * 32768 + 4 * r2 * 1024 + lane * 16;
  unsigned d000 = sb + slot * 32768 + 16384 + (8 * q + 4 * (q & 1) + r4) * 256 + (((8 * c2 + p) ^ swz) * 16) + (p & 1) * 8;
  unsigned d001 = d000 ^ 8, d010 = d000 ^ 64, d011 = d000 ^ 72, d100 = d000 ^ 1024, d101 = d000 ^ 1032, d110 = d000 ^ 1088, d111 = d000 ^ 1096;
#define W0(e) cat(w0[e])
#define W1(e) cat(w1[e])
#define TIEW(wc) TIE4(wc[0].lo, wc[0].hi, wc[1].lo, wc[1].hi); TIE4(wc[2].lo, wc[2].hi, wc[3].lo, wc[3].hi)
#define KD(BC, TIEB, wn, zc, zn, KO, SYNC, X10, X11, X12, X14, X15) \
    TIEB; TIE2(fa[0], fa[1]); TIE1(zc); WL(1); SCHED(); \
    G4(0, fa[0], BC, , , SYNC, DTR(wn[0].lo, d000, KO); DTR(wn[0].hi, d100, KO)); \
    G4(1, fa[1], BC, DTR(wn[1].lo, d001, KO); DTR(wn[1].hi, d101, KO), DSR(fa[0], hD, KO), DTR(wn[2].lo, d010, KO); DTR(wn[2].hi, d110, KO), \
       DTR(wn[3].lo, d011, KO); DTR(wn[3].hi, d111, KO); TIE1(fa[2]); WL(9)); \
    G4(2, fa[2], BC, DSR(zn, hD, KO + 3072), DSR(fa[1], hD, KO + 1024), X10, X11); \
    G4(3, zc, BC, X12, DSR(fa[2], hD, KO + 2048), X14, X15);
  KC(FB0, fb0, fb1, fz0, fz1, aC1, 8192, , , , EN, EN, 0);
  KD(FB1, TIE4(fb1[0], fb1[1], fb1[2], fb1[3]), w0, fz1, fz0, 0, WL(0); BAR(), , , , , );
  STAMP(16);
  {
    float gt[4];
#pragma unroll
    for (int i = 0; i < 4; ++i) gt[i] = 1.f - __builtin_amdgcn_rcpf(c2 ? cz[i ^ 2] : cz[i]);
#pragma unroll
    for (int i = 0; i < 4; ++i)
#pragma unroll
      for (int e = 0; e < 4; ++e) acc[i][e] *= gt[i];
    SCHED();
  }
#pragma unroll 1
  for (int t = 0; t < 6; ++t) {
    const int dl = slot == 4 ? -4 * 32768 : 32768;
    slot = slot == 4 ? 0 : slot + 1;
    KD(W0, TIEW(w0), w1, fz0, fz1, 8192, , ADV(d000, dl); ADV(d100, dl), ADV(d001, dl); ADV(d101, dl), ADV(d010, dl); ADV(d110, dl), ADV(d011, dl); ADV(d111, dl), ADV(hD, dl));
    KD(W1, TIEW(w1), w0, fz1, fz0, 0, WL(0); BAR(), , , , , );
  }
  KD(W0, TIEW(w0), w1, fz0, fz1, 8192, , , , , , );
  TIEW(w1); TIE4(fa[0], fa[1], fa[2], fz1); WL(0); SCHED();
  G4(1, fa[1], W1, , , , ); G4(3, fz1, W1, , , , ); G4(2, fa[2], W1, , , , ); G4(0, fa[0], W1, , , , );
  STAMP(17);

#pragma unroll
  for (int i = 1; i < 4; ++i)
#pragma unroll
    for (int h = 0; h < 2; ++h) {
      u32x4 v = {pk_bf16(acc[i][2 * h][0], acc[i][2 * h][1]), pk_bf16(acc[i][2 * h][2], acc[i][2 * h][3]),
                 pk_bf16(acc[i][2 * h + 1][0], acc[i][2 * h + 1][1]), pk_bf16(acc[i][2 * h + 1][2], acc[i][2 * h + 1][3])};
#pragma unroll
      for (int k = 0; k < 4; ++k) v[k] = min(v[k], 0xfffffffeu);
      st_wt(v, rp, lane * 16 + h * 1024, (((s ^ i) * 3 + (i - 1)) * 4 + w) * 2048);
    }
  STAMP(18);
  u32x4 pv[6];
  for (unsigned spins = 0; spins < (1u << 16); ++spins) {
#pragma unroll
    for (int k = 0; k < 6; ++k) pv[k] = __builtin_amdgcn_raw_buffer_load_b128(rp, lane * 16 + (k & 1) * 1024, ((s * 3 + (k >> 1)) * 4 + w) * 2048, 16);
    unsigned m = 0;
#pragma unroll
    for (int k = 0; k < 6; ++k) m = max(max(m, max(pv[k][0], pv[k][1])), max(pv[k][2], pv[k][3]));
    if (__all(m != 0xffffffffu)) break;
    asm volatile("" ::: "memory");
  }
  STAMP(19);
#pragma unroll
  for (int k = 0; k < 6; ++k)
#pragma unroll
    for (int d = 0; d < 4; ++d) {
      const int e = 2 * (k & 1) + (d >> 1), r = 2 * (d & 1);
      acc[0][e][r] += __builtin_bit_cast(float, pv[k][d] << 16);
      acc[0][e][r + 1] += __builtin_bit_cast(float, pv[k][d] & 0xffff0000u);
    }
  u16* op = out + (size_t)(rb * 128 + (4 * r2 + s) * 16 + rho) * D_ + nb * 128 + 64 * c2 + 8 * q;
#pragma unroll
  for (int h = 0; h < 2; ++h) {
    const unsigned a0 = pk_bf16(acc[0][2 * h][0], acc[0][2 * h][1]), a1 = pk_bf16(acc[0][2 * h][2], acc[0][2 * h][3]);
    const unsigned b0 = pk_bf16(acc[0][2 * h + 1][0], acc[0][2 * h + 1][1]), b1 = pk_bf16(acc[0][2 * h + 1][2], acc[0][2 * h + 1][3]);
    const bool o = q & 1;
    const u32x4 v = {o ? b0 : a0, o ? b1 : a1, o ? a0 : b0, o ? a1 : b1};
    *(u32x4*)(op + 32 * h) = v;
  }
  STAMP(20);
}

extern "C" void kernel_launch(void* const* d_in, const int* in_sizes, int n_in, void* d_out, int out_size, void* d_ws,
                              size_t ws_size, hipStream_t stream) {
  (void)hipMemsetAsync((char*)d_ws + WS_FLAG, 0, 16384, stream);
  fused<<<256, 512, 0, stream>>>((const u16*)d_in[0], (const u16*)d_in[1], (const u16*)d_in[2], (const u16*)d_in[3], (const u16*)d_in[4],
                                 (const u16*)d_in[6], (const u16*)d_in[7], (const u16*)d_in[8], (const u16*)d_in[9], (char*)d_ws, (u16*)d_out);
}
```

```cpp
#include <hip/hip_runtime.h>
#include <stdint.h>

#define LAS __attribute__((address_space(3)))
#define GAS __attribute__((address_space(1)))
#define DI __device__ __forceinline__
typedef unsigned short u16;
typedef __bf16 bf16x8 __attribute__((ext_vector_type(8)));
typedef __bf16 bf16x4 __attribute__((ext_vector_type(4)));
typedef __bf16 bf16x2 __attribute__((ext_vector_type(2)));
typedef float f32x4 __attribute__((ext_vector_type(4)));
typedef unsigned u32x4 __attribute__((ext_vector_type(4)));

constexpr int T_ = 2048, D_ = 512, F_ = 1792;
constexpr size_t WS_FLAG = 0;
constexpr size_t WS_ABF = 1u << 20;
constexpr size_t WS_HF = 8u << 20;
constexpr size_t WS_PART = 16u << 20;
constexpr int L_GW = 147456, L_Z = L_GW + 2048, L_TCH = L_GW + 4096, L_SIZE = 163840;

#define HAUX 16
#define INV() asm volatile("" ::: "memory")
#define STAG 2
#define TCH 0
#define WARM 0
#define LFIRST 1
#define DROT 2
#define EARLY 0
#define FBT 7
#define STB 2
#define STC 2
#define EB 1
#define POLL2 0
#define GETP 1
#define EXP 0
#define MFMA(a, b, c) __builtin_amdgcn_mfma_f32_16x16x32_bf16((a), (b), (c), 0, 0, 0)
#define WAIT_V(n) asm volatile("s_waitcnt vmcnt(%0)" ::"n"(n) : "memory")
#define BAR() __builtin_amdgcn_s_barrier()
#define SCHED() __builtin_amdgcn_sched_barrier(0)
#define DSR(dst, addr, off) asm volatile("ds_read_b128 %0, %1 offset:%2" : "=v"(dst) : "v"(addr), "i"(off))
#define DSR32(dst, addr, off) asm volatile("ds_read_b32 %0, %1 offset:%2" : "=v"(dst) : "v"(addr), "i"(off))
#define DTR(dst, addr, off) asm volatile("ds_read_b64_tr_b16 %0, %1 offset:%2" : "=v"(dst) : "v"(addr), "i"(off))
#define ADV(x, d) asm volatile("v_add_u32 %0, %1, %0" : "+v"(x) : "s"(d))
#define WL(n) asm volatile("s_waitcnt lgkmcnt(%0)" ::"n"(n))
#define TIE1(a) asm volatile("" : "+v"(a))
#define TIE2(a, b) asm volatile("" : "+v"(a), "+v"(b))
#define TIE4(a, b, c, d) asm volatile("" : "+v"(a), "+v"(b), "+v"(c), "+v"(d))

#define STAMP(i)
#define STAMPR(i)

struct TF { bf16x4 lo, hi; };
DI bf16x8 cat(const TF& f) { return __builtin_shufflevector(f.lo, f.hi, 0, 1, 2, 3, 4, 5, 6, 7); }
DI unsigned pk_bf16(float lo, float hi) { bf16x2 v; v[0] = (__bf16)lo; v[1] = (__bf16)hi; return __builtin_bit_cast(unsigned, v); }
DI __amdgpu_buffer_rsrc_t mk_rsrc(const void* p, unsigned bytes) { return __builtin_amdgcn_make_buffer_rsrc((void*)p, 0, bytes, 0x00020000); }
#define NT 0
template <int B> DI void glds(__amdgpu_buffer_rsrc_t r, LAS char* dst, int voff, int soff) {
  __builtin_amdgcn_raw_ptr_buffer_load_lds(r, (LAS void*)dst, 16, voff, soff, 0, (NT >> B & 1) * 2);
}
DI void glds1(__amdgpu_buffer_rsrc_t r, LAS char* dst, int voff, int soff) {
  __builtin_amdgcn_raw_ptr_buffer_load_lds(r, (LAS void*)dst, 16, voff, soff, 0, HAUX);
}
DI void st_wt(u32x4 v, __amdgpu_buffer_rsrc_t r, int voff, int soff) { __builtin_amdgcn_raw_buffer_store_b128(v, r, voff, soff, 16); }
#define TH 0
#define TW 3
template <int N> struct IC { static constexpr int v = N; };
template <int I, class T> DI const T& pick(const T& a0, const T& a1, const T& a2, const T& a3, const T& a4, const T& a5, const T& a6) {
  if constexpr (I == 0) return a0; else if constexpr (I == 1) return a1; else if constexpr (I == 2) return a2; else if constexpr (I == 3) return a3;
  else if constexpr (I == 4) return a4; else if constexpr (I == 5) return a5; else return a6;
}

#define TLBW 7
#define TLBP 0
#define TLBM 7
#define TLB_TOUCH() do { \
    const int g_ = lane >> 3, i_ = lane & 7; \
    LAS void* td_ = (LAS void*)(smem + L_TCH + w * 256); \
    if (w == 0) { \
      if (TLBW & 1) { \
        const size_t co_ = ((size_t)rb * 128 * T_ + s * 512) * 2; \
        const char* tp_ = (g_ == 0 ? (const char*)Cr + co_ : g_ == 1 ? (const char*)Ci + co_ : g_ == 2 ? (const char*)Wd + (448 * s * 1024 + nb * 256) : (const char*)out + (rb * 131072 + nb * 256)) + i_ * 65536; \
        if (i_ < (g_ < 2 ? 8 : g_ == 2 ? 7 : g_ == 3 ? 2 : 0) && (i_ & TLBM) == 0) __builtin_amdgcn_global_load_lds((GAS const void*)tp_, td_, 4, 0, 0); \
      } \
    } else if (w == 1) { \
      if (TLBW & 2) { \
        const char* tp_ = ws + (g_ == 0 ? WS_ABF + (size_t)(s * 4 + nb) * 262144 : g_ == 1 ? WS_ABF + (size_t)((rb >> 2) * 4 + ((cb & 7) >> 1)) * 262144 + (cb & 8) * 16384 \
                              : g_ == 2 ? WS_HF + (size_t)(rb * 4 + s) * 114688 : g_ == 3 ? WS_PART + (size_t)tile * 98304 : WS_FLAG) + i_ * 65536; \
        if (i_ < (g_ == 0 ? 4 : g_ < 4 ? 2 : g_ == 4 ? 1 : 0) && (i_ & TLBM) == 0) __builtin_amdgcn_global_load_lds((GAS const void*)tp_, td_, 4, 0, 16); \
      } \
    } else if (TLBW & 4) { \
      const char* tp_ = (const char*)(w == 2 ? Wg : Wu) + cb * 224 + lane * 65536 / 3584 * 3584; \
      if (lane < 28 && (i_ & TLBM) == 0) __builtin_amdgcn_global_load_lds((GAS const void*)tp_, td_, 4, 0, 0); \
    } \
  } while (0)
#define CODEW 2
#define CORD 2
#define AORD 0
#define BORD 0
#define CWV 3
extern "C" __global__ __launch_bounds__(512, 2) void fused(const u16* __restrict__ x, const u16* __restrict__ Cr, const u16* __restrict__ Ci,
                                                const u16* __restrict__ Hr, const u16* __restrict__ Hi, const u16* __restrict__ Wg,
                                                const u16* __restrict__ Wu, const u16* __restrict__ Wd, const u16* __restrict__ gwp,
                                                char* __restrict__ ws, u16* __restrict__ out) {
  __shared__ __attribute__((aligned(1024))) char smem_[L_SIZE];
  LAS char* smem = (LAS char*)smem_;
  const int id = blockIdx.x, xcd = id & 7, idx = id >> 3;
  const int rb = (xcd & 1) * 8 + (idx >> 2), j = idx & 3, s = xcd >> 1, cb = 4 * s + j, nb = j;
  const int wid = __builtin_amdgcn_readfirstlane(threadIdx.x >> 6);
#define LANE(v) int v; asm volatile("v_mbcnt_lo_u32_b32 %0, -1, 0\n\tv_mbcnt_hi_u32_b32 %0, -1, %0" : "=v"(v))
    if (wid == CWV) {
      LANE(lane);
      unsigned long long pc_; unsigned o_, n_;
      unsigned n2_;
      asm volatile("s_getpc_b64 %0\n.Lkpc%=:\n\ts_mov_b32 %1, .Lkpc%=-fused\n\ts_mov_b32 %2, .Lkea-fused\n\ts_mov_b32 %3, .Lkeb-fused" : "=s"(pc_), "=s"(o_), "=s"(n_), "=s"(n2_));
      n_ = max(n_, n2_);
      const unsigned l_ = CODEW == 1 ? ((idx & 3) * 64 + lane) * 128 : ((idx & 7) * 32 + (lane & 31)) * 128;
      if (l_ < n_ - 4 && (CODEW == 1 || lane < 32)) __builtin_amdgcn_global_load_lds((GAS const void*)(pc_ - o_ + l_), (LAS void*)(smem + L_TCH + 1024), 4, 0, 0);
    }
#define ROT(c) (c == 12 ? ((rb & 3) + 2 * j) & 7 : c == 11 ? (2 * (rb & 3) + (j & 1)) : c == 10 ? (rb >> 1 & 3) : c == 9 ? (rb & 1) : c == 8 ? (4 * (rb & 1)) : c == 7 ? ((rb & 3) + 4 * (j & 1)) : c == 6 ? (2 * (rb & 3)) \
              : c == 5 ? (4 * (rb & 1) + 2 * (j & 1)) : c == 4 ? (2 * j + (rb & 1)) : c == 3 ? ((rb + j) & 7) : c == 2 ? ((rb + 2 * j) & 7) : c ? (rb & 7) : 0)
  const int off = ROT(STAG), offb = ROT(STB), offc = ROT(STC);
#define UN(m, o, t) (m == 1 ? ((t) & 4) + (((t) + j + rb) & 3) : m == 2 ? ((((t) >> 2) + (rb >> 2)) & 1) * 4 + (((t) + j + rb) & 3) : (((t) + o) & 7))
  STAMPR(0);

  if (wid < 4) {
  LANE(lane);
  const int w = wid, r2 = w >> 1, c2 = w & 1;
  const int rho = lane & 15, q = lane >> 4, r4 = rho >> 2, p = lane & 3;
  const unsigned sb = (unsigned)(size_t)smem;
  const float L2E = 1.4426950408889634f;
  const int tile = rb * 4 + nb;
  const __amdgpu_buffer_rsrc_t rp = mk_rsrc(ws + WS_PART + (size_t)tile * 98304, 98304);
  const unsigned rowp = rho * 128 + ((q ^ (rho >> 1)) * 16);
  const unsigned swz = (r4 & 1) | ((r4 >> 1) << 2) | ((q & 1) << 3);

  unsigned xB00 = sb + 98304 + (4 * r2 + 2 * c2) * 2048 + rowp, xB01 = xB00 ^ 4096, xB10 = xB00 ^ 64, xB11 = xB01 ^ 64;
  const unsigned wrow = sb + 98304 + 16384 + (8 * q + r4) * 256;
  unsigned wO0 = wrow + (((4 * c2 + p) ^ swz) * 16) + (p & 1) * 8, wO1 = wO0 ^ 8;
  unsigned wP0 = wrow + (((8 + p) ^ swz) * 16) + (p & 1) * 8, wP1 = wP0 ^ 8;
  const unsigned gam = (0x2130u >> (4 * p)) & 3u;
  unsigned wS = wrow + (((12 + (gam >> 1)) ^ swz) * 16) + (gam & 1) * 8;
  bf16x8 xc[4], xn[4];
  TF O0g, O0u, O1g, O1u, P0g, P0u, P1g, P1u, Sg0, Su0, Sg1, Su1;
  f32x4 gO0[4] = {}, uO0[4] = {}, gO1[4] = {}, uO1[4] = {}, gP0[2] = {}, uP0[2] = {}, gP1[2] = {}, uP1[2] = {}, gS[2] = {}, uS[2] = {};
  TIE4(gO0[0], gO0[1], gO0[2], gO0[3]); TIE4(uO0[0], uO0[1], uO0[2], uO0[3]); TIE4(gO1[0], gO1[1], gO1[2], gO1[3]); TIE4(uO1[0], uO1[1], uO1[2], uO1[3]);
  TIE4(gP0[0], gP0[1], uP0[0], uP0[1]); TIE4(gP1[0], gP1[1], uP1[0], uP1[1]); TIE4(gS[0], gS[1], uS[0], uS[1]);

  {
    struct FA { bf16x8 x0, x1, h0, h1, h2, h3, g; };
    FA f0, f1;
    unsigned xA0 = sb + 4096 * w + rowp, xA1 = xA0 ^ 64, hA0 = sb + 16384 + rowp, hA1 = hA0 ^ 64, gA = sb + L_GW + q * 16 + UN(AORD, off, 0) * 128;
    f32x4 ac[2][4] = {}, z0 = {}, z1 = {};
    TIE4(ac[0][0], ac[0][1], ac[0][2], ac[0][3]); TIE4(ac[1][0], ac[1][1], ac[1][2], ac[1][3]); TIE2(z0, z1);
#define KA_(fc, G0, G1, G2, G3, G4, G5, G6, G7, G8) \
    ac[0][0] = MFMA(fc.x0, fc.h0, ac[0][0]); SCHED(); G0; SCHED(); \
    ac[1][0] = MFMA(fc.x1, fc.h0, ac[1][0]); SCHED(); G1; SCHED(); \
    ac[0][1] = MFMA(fc.x0, fc.h1, ac[0][1]); SCHED(); G2; SCHED(); \
    ac[1][1] = MFMA(fc.x1, fc.h1, ac[1][1]); SCHED(); G3; SCHED(); \
    ac[0][2] = MFMA(fc.x0, fc.h2, ac[0][2]); SCHED(); G4; SCHED(); \
    ac[1][2] = MFMA(fc.x1, fc.h2, ac[1][2]); SCHED(); G5; SCHED(); \
    ac[0][3] = MFMA(fc.x0, fc.h3, ac[0][3]); SCHED(); G6; SCHED(); \
    ac[1][3] = MFMA(fc.x1, fc.h3, ac[1][3]); SCHED(); G7; SCHED(); \
    z0 = MFMA(fc.x0, fc.g, z0); SCHED(); G8; SCHED(); \
    z1 = MFMA(fc.x1, fc.g, z1); SCHED();
#define KA(...) KA_(__VA_ARGS__)
#define KA0(X7, X8) \
    TIE4(f0.x0, f0.x1, f0.h0, f0.h1); WL(3); SCHED(); \
    KA(f0, DSR(f1.x0, xA1, 0), DSR(f1.h0, hA1, 0), DSR(f1.x1, xA1, 2048), DSR(f1.h1, hA1, 2048); TIE2(f0.h2, f0.h3); TIE1(f0.g); WL(4), \
       DSR(f1.h2, hA1, 4096), DSR(f1.h3, hA1, 6144), DSR(f1.g, gA, 64), X7, X8)
#define KA1_HEAD TIE4(f1.x0, f1.x1, f1.h0, f1.h1); WL(3); SCHED();
#define KA1_SYNC TIE2(f1.h2, f1.h3); TIE1(f1.g); WL(0); BAR()
    if (TLBP == 0) TLB_TOUCH();
    BAR();
    STAMP(1);
    if (TLBP == 1) TLB_TOUCH();
    DSR(f0.x0, xA0, 0); DSR(f0.h0, hA0, 0); DSR(f0.x1, xA0, 2048); DSR(f0.h1, hA0, 2048); DSR(f0.h2, hA0, 4096); DSR(f0.h3, hA0, 6144); DSR(f0.g, gA, 0);
#pragma unroll 1
    for (int t = 0; t < 7; ++t) {
      const int dl = t == 4 ? -98304 : t == 5 ? 49152 : 24576, dh = t == 3 ? 65536 : t == 4 ? -139264 : dl, dg = (UN(AORD, off, t + 1) - UN(AORD, off, t)) * 128;
      KA0(ADV(xA0, dl); ADV(hA0, dh), ADV(gA, dg));
      KA1_HEAD;
      KA(f1, ADV(xA1, dl); ADV(hA1, dh), KA1_SYNC; DSR(f0.x0, xA0, 0), DSR(f0.h0, hA0, 0), DSR(f0.x1, xA0, 2048), DSR(f0.h1, hA0, 2048),
         DSR(f0.h2, hA0, 4096), DSR(f0.h3, hA0, 6144), DSR(f0.g, gA, 0), );
    }
    KA0(, );
    KA1_HEAD;
    KA(f1, , KA1_SYNC; DSR(xc[0], xB00, 0); DSR(xc[1], xB00, 2048),
           DTR(O0g.lo, wO0, 0); DTR(O0g.hi, wO0, 1024); DSR(xc[2], xB01, 0),
           DSR(xc[3], xB01, 2048); DTR(O0u.lo, wO0, 16384); DTR(O0u.hi, wO0, 17408),
           DTR(Sg0.lo, wS, 0); DTR(Sg0.hi, wS, 1024); DTR(O1g.lo, wO1, 0),
           DTR(O1g.hi, wO1, 1024); DTR(Su0.lo, wS, 16384); DTR(Su0.hi, wS, 17408),
           DTR(O1u.lo, wO1, 16384); DTR(O1u.hi, wO1, 17408); DTR(P0g.lo, wP0, 0),
           DTR(P0g.hi, wP0, 1024); DTR(P0u.lo, wP0, 16384); DTR(P0u.hi, wP0, 17408),
           DTR(P1g.lo, wP1, 0); DTR(P1g.hi, wP1, 1024); DTR(P1u.lo, wP1, 16384); DTR(P1u.hi, wP1, 17408));
    STAMP(2);
    if ((TLBW) + (CODEW)) WAIT_V(0);
    const unsigned sg = cb < 8 ? 0u : 0x80008000u;
    const __amdgpu_buffer_rsrc_t ra = mk_rsrc(ws + WS_ABF + (size_t)(((rb >> 2) * 4 + ((cb & 7) >> 1)) * 16 + (cb & 8) + 2 * (rb & 3) + r2) * 16384 + (c2 * 8 + 4 * (cb & 1)) * 1024, 4096);
#pragma unroll
    for (int e = 0; e < 4; ++e) {
      u32x4 v = {pk_bf16(ac[0][e][0], ac[0][e][1]) ^ sg, pk_bf16(ac[0][e][2], ac[0][e][3]) ^ sg, pk_bf16(ac[1][e][0], ac[1][e][1]) ^ sg, pk_bf16(ac[1][e][2], ac[1][e][3]) ^ sg};
      st_wt(v, ra, lane * 16, e * 1024);
    }
    const unsigned zw = sb + L_Z + (32 * w + 8 * q + 4 * (rho & 1)) * 4;
    const f32x4 zs = (rho & 1) ? z1 : z0;
    if (rho < 2) asm volatile("ds_write_b128 %0, %1" ::"v"(zw), "v"(zs));
    SCHED();
    STAMP(3);
  }

  float cz[4];
  bf16x8 fa[3], fz0, fz1, fb0[4], fb1[4];
  unsigned aC0 = sb + 65536 + 4 * r2 * 2048 + rowp, aC1 = aC0 ^ 64, bC = sb + 65536 + 16384 + 4 * c2 * 1024 + lane * 16;
#define M4(acc, W, X, G0, G1, G2, G3) \
    acc[0] = MFMA(cat(W), X[0], acc[0]); SCHED(); G0; SCHED(); \
    acc[1] = MFMA(cat(W), X[1], acc[1]); SCHED(); G1; SCHED(); \
    acc[2] = MFMA(cat(W), X[2], acc[2]); SCHED(); G2; SCHED(); \
    acc[3] = MFMA(cat(W), X[3], acc[3]); SCHED(); G3; SCHED();
#define M2(acc, W, X, G0, G1) \
    acc[0] = MFMA(cat(W), X[0], acc[0]); SCHED(); G0; SCHED(); \
    acc[1] = MFMA(cat(W), X[1], acc[1]); SCHED(); G1; SCHED();
#define TIEF(f) TIE2(f.lo, f.hi)
#define KB(X, XN, SG, SU, SGN, SUN, XL, XH, KO, G0, G1, G2, G27, Z, NW8) \
    TIE4(X[0], X[1], X[2], X[3]); TIEF(O0g); TIEF(O0u); WL(15); SCHED(); \
    M4(gO0, O0g, X, G0, G1, G2, DSR(XN[0], XL, 0)); \
    M4(uO0, O0u, X, DSR(XN[1], XL, 2048), DTR(O0g.lo, wO0, KO), DTR(O0g.hi, wO0, KO + 1024), DSR(XN[2], XH, 0); TIEF(O1g); TIEF(O1u); Z(-1); WL(NW8)); \
    M4(gO1, O1g, X, DSR(XN[3], XH, 2048); Z(0), DTR(O0u.lo, wO0, KO + 16384); Z(1), DTR(O0u.hi, wO0, KO + 17408); Z(2), DTR(SGN.lo, wS, KO); Z(3)); \
    M4(uO1, O1u, X, DTR(SGN.hi, wS, KO + 1024); Z(4), DTR(O1g.lo, wO1, KO); Z(5), DTR(O1g.hi, wO1, KO + 1024); Z(6), DTR(SUN.lo, wS, KO + 16384); Z(7); TIEF(P0g); TIEF(P0u); WL(15)); \
    M2(gP0, P0g, X, DTR(SUN.hi, wS, KO + 17408); Z(8), DTR(O1u.lo, wO1, KO + 16384); Z(9)); \
    M2(uP0, P0u, X, DTR(O1u.hi, wO1, KO + 17408); Z(10), DTR(P0g.lo, wP0, KO); Z(11); TIEF(P1g); TIEF(P1u); TIEF(SG); TIEF(SU); WL(15)); \
    M2(gP1, P1g, X, DTR(P0g.hi, wP0, KO + 1024); Z(12), DTR(P0u.lo, wP0, KO + 16384); Z(13)); \
    M2(uP1, P1u, X, DTR(P0u.hi, wP0, KO + 17408); Z(14), DTR(P1g.lo, wP1, KO); Z(15)); \
    M2(gS, SG, X, DTR(P1g.hi, wP1, KO + 1024), DTR(P1u.lo, wP1, KO + 16384)); \
    M2(uS, SU, X, DTR(P1u.hi, wP1, KO + 17408), G27);
#define ZN(k)
  const int kap = 7 * (j >> 1) + 4 * (j & 1), sg2 = s ^ (2 * c2);
  const __amdgpu_buffer_rsrc_t rh = mk_rsrc(ws + WS_HF + (size_t)(rb * 4 + s) * 114688, 114688);
  float et[8];
  u32x4 pc;
  auto eop = [&](auto N) {
    constexpr int n = decltype(N)::v, pi = n / 46, o = n % 46;
    if constexpr (pi < 7) {
      if constexpr (o < 40) {
        constexpr int grp = o / 20, r = o % 20, st = r / 4, e = r & 3, v = grp * 4 + e;
        const f32x4& g = grp ? pick<pi>(gO1[0], gO1[1], gO1[2], gO1[3], gP1[0], gP1[1], gS[1]) : pick<pi>(gO0[0], gO0[1], gO0[2], gO0[3], gP0[0], gP0[1], gS[0]);
        const f32x4& u = grp ? pick<pi>(uO1[0], uO1[1], uO1[2], uO1[3], uP1[0], uP1[1], uS[1]) : pick<pi>(uO0[0], uO0[1], uO0[2], uO0[3], uP0[0], uP0[1], uS[0]);
        const float c = cz[pi < 4 ? pi : pi < 6 ? pi - 4 : grp];
        if constexpr (st == 0) et[v] = g[e] * -L2E;
        else if constexpr (st == 1) et[v] = __builtin_amdgcn_exp2f(et[v]);
        else if constexpr (st == 2) et[v] = __builtin_fmaf(et[v], c, c);
        else if constexpr (st == 3) et[v] = __builtin_amdgcn_rcpf(et[v]);
        else et[v] = et[v] * u[e];
      } else if constexpr (o < 44) {
        pc[o - 40] = pk_bf16(et[2 * (o - 40)], et[2 * (o - 40) + 1]);
      } else if constexpr (o == 44) {
        if constexpr (pi == 6) {
          auto s0 = __builtin_amdgcn_permlane32_swap(pc[0], pc[2], false, false);
          auto s1 = __builtin_amdgcn_permlane32_swap(pc[1], pc[3], false, false);
          pc[0] = s0[0]; pc[2] = s0[1]; pc[1] = s1[0]; pc[3] = s1[1];
        }
      } else {
        if constexpr (pi == 6) st_wt(pc, rh, (sg2 ^ (q >> 1)) * 1024 + (((q & 1) + 2 * (j & 1)) * 16 + rho) * 16, (7 * (j >> 1) + 3) * 8192 + 4 * r2 * 1024);
        else st_wt(pc, rh, lane * 16, (kap + (pi < 4 ? c2 : 2)) * 8192 + (4 * r2 + (sg2 ^ (pi < 4 ? pi : pi - 4))) * 1024);
      }
    }
  };
#define E1(n) eop(IC<(n)>{})
#define E2(n) eop(IC<(n)>{}); eop(IC<(n) + 1>{})
  {
    int reg = 2;
    const unsigned zA = sb + L_Z + (64 * r2 + rho) * 4 + sg2 * 64;
#define ZRD() if (t == 6) asm volatile("v_xor_b32 %1, 64, %4\n\tv_xor_b32 %2, 0x80, %4\n\tv_xor_b32 %3, 0xc0, %4\n\tds_read_b32 %0, %4\n\tds_read_b32 %1, %1\n\t" \
      "ds_read_b32 %2, %2\n\tds_read_b32 %3, %3" : "=&v"(cz[0]), "=&v"(cz[1]), "=&v"(cz[2]), "=&v"(cz[3]) : "v"(zA))
    const u32x4 empty = {0xffffffffu, 0xffffffffu, 0xffffffffu, 0xffffffffu};
#pragma unroll 1
    for (int t = 0; t < 7; ++t) {
      const int dl = reg == 2 ? -2 * 49152 : 49152;
      reg = reg == 2 ? 0 : reg + 1;
      KB(xc, xn, Sg0, Su0, Sg1, Su1, xB10, xB11, 8192, ADV(xB00, dl), ADV(xB01, dl), , ADV(wO0, dl); ADV(wO1, dl), ZN, 13);
      KB(xn, xc, Sg1, Su1, Sg0, Su0, xB00, xB01, 0, ZRD(); ADV(wP0, dl); ADV(wP1, dl), ADV(wS, dl); ADV(xB10, dl), ADV(xB11, dl); WL(0); BAR(), , ZN, 13);
      if (t == 1) {
        WAIT_V(1);
        if (lane == 0) __hip_atomic_store((GAS unsigned*)(ws + WS_FLAG) + (rb * 16 + cb) * 16 + w, 1u, __ATOMIC_RELAXED, __HIP_MEMORY_SCOPE_AGENT);
      }
      const int ti = t < 5 ? t : 5;
      st_wt(empty, rp, lane * 16, ((s * 3 + (ti >> 1)) * 4 + w) * 2048 + (ti & 1) * 1024);
      SCHED();
      if (t & 1) STAMP(4 + t);
    }
    auto zop = [&](auto N) {
      constexpr int n = decltype(N)::v, st = n >> 2, v = n & 3;
      if constexpr (n == -1) { TIE4(cz[0], cz[1], cz[2], cz[3]); }
      else if constexpr (st == 0) cz[v] *= L2E;
      else if constexpr (st == 1) cz[v] = __builtin_amdgcn_exp2f(cz[v]);
      else if constexpr (st == 2) cz[v] += 1.f;
      else cz[v] = fminf(cz[v], 3.0e38f);
    };
    SCHED();
#define ZQ(k) zop(IC<k>{})
    KB(xc, xn, Sg0, Su0, Sg1, Su1, xB10, xB11, 8192, , , , , ZQ, 13);
    TIE4(xn[0], xn[1], xn[2], xn[3]); TIEF(O0g); TIEF(O0u); WL(15); SCHED();
    M4(gO0, O0g, xn, , , TIEF(O1g); TIEF(O1u); TIEF(P0g); TIEF(P0u); TIEF(P1g); TIEF(P1u); TIEF(Sg1); TIEF(Su1); WL(0); BAR(), DSR(fb0[0], bC, 0));
    M4(uO0, O0u, xn, DSR(fb0[1], bC, 1024), DSR(fa[0], aC0, 0), DSR(fb0[2], bC, 2048), DSR(fb0[3], bC, 3072));
    M4(gO1, O1g, xn, DSR(fz0, aC0, 6144), DSR(fa[1], aC0, 2048), DSR(fa[2], aC0, 4096), E2(0));
    M4(uO1, O1u, xn, E2(2), E2(4), E2(6), E2(8));
    M2(gP0, P0g, xn, E2(10), E2(12));
    M2(uP0, P0u, xn, E2(14), E2(16));
    M2(gP1, P1g, xn, E2(18), E2(20));
    M2(uP1, P1u, xn, E2(22), E2(24));
    M2(gS, Sg1, xn, E2(26), E2(28));
    M2(uS, Su1, xn, E2(30), E2(32));
  }
  STAMP(12);

  f32x4 acc[4][4] = {};
#define G4(i, A, B, G0, G1, G2, G3) \
    acc[i][0] = MFMA(B(0), A, acc[i][0]); SCHED(); G0; SCHED(); \
    acc[i][1] = MFMA(B(1), A, acc[i][1]); SCHED(); G1; SCHED(); \
    acc[i][2] = MFMA(B(2), A, acc[i][2]); SCHED(); G2; SCHED(); \
    acc[i][3] = MFMA(B(3), A, acc[i][3]); SCHED(); G3; SCHED();
#define FB0(e) fb0[e]
#define FB1(e) fb1[e]
#define KC(BC, bc, bn, zc, zn, AA, BO, SYNC, X10, X11, e1, e2, e0) \
    TIE4(bc[0], bc[1], bc[2], bc[3]); TIE2(fa[0], fa[1]); TIE1(zc); WL(1); SCHED(); \
    G4(0, fa[0], BC, e2(e0 + 0), e2(e0 + 2), SYNC; e2(e0 + 4), DSR(bn[0], bC, BO); e1(e0 + 6)); \
    G4(1, fa[1], BC, DSR(bn[1], bC, BO + 1024); e1(e0 + 7), DSR(fa[0], AA, 0); e1(e0 + 8), DSR(bn[2], bC, BO + 2048); e1(e0 + 9), DSR(bn[3], bC, BO + 3072); e1(e0 + 10); TIE1(fa[2]); WL(5)); \
    G4(2, fa[2], BC, DSR(zn, AA, 6144); e1(e0 + 11), DSR(fa[1], AA, 2048); e1(e0 + 12), X10; e2(e0 + 13), X11; e2(e0 + 15)); \
    G4(3, zc, BC, e2(e0 + 17), DSR(fa[2], AA, 4096); e1(e0 + 19), e2(e0 + 20), e2(e0 + 22));
#define STAGEC(e1, e2, e0) { \
    const int dl = slot == 4 ? -4 * 32768 : 32768; \
    slot = slot == 4 ? 0 : slot + 1; \
    KC(FB0, fb0, fb1, fz0, fz1, aC1, 8192, , ADV(aC0, dl), ADV(bC, dl), e1, e2, e0); \
    KC(FB1, fb1, fb0, fz1, fz0, aC0, 0, WL(0); BAR(), ADV(aC1, dl), , e1, e2, e0 + 24); }
  int slot = 2;
  STAGEC(E1, E2, 34); STAGEC(E1, E2, 82); STAGEC(E1, E2, 130);
  STAMP(13);
  STAGEC(E1, E2, 178); STAGEC(E1, E2, 226); STAGEC(E1, E2, 274);
  STAMP(14);
#define EN(k)
#pragma unroll 1
  for (int t = 6; t < 15; ++t) {
    STAGEC(EN, EN, 0);
    if (t == FBT) {
      WAIT_V(0);
      if (lane == 0) __hip_atomic_store((GAS unsigned*)(ws + WS_FLAG) + (rb * 16 + cb) * 16 + 4 + w, 1u, __ATOMIC_RELAXED, __HIP_MEMORY_SCOPE_AGENT);
      SCHED();
    }
    if (t == 10) STAMP(15);
  }
  TF w0[4], w1[4];
  slot = slot == 4 ? 0 : slot + 1;
  unsigned hD = sb + slot * 32768 + 4 * r2 * 1024 + lane * 16;
  unsigned d000 = sb + slot * 32768 + 16384 + (8 * q + 4 * (q & 1) + r4) * 256 + (((8 * c2 + p) ^ swz) * 16) + (p & 1) * 8;
  unsigned d001 = d000 ^ 8, d010 = d000 ^ 64, d011 = d000 ^ 72, d100 = d000 ^ 1024, d101 = d000 ^ 1032, d110 = d000 ^ 1088, d111 = d000 ^ 1096;
#define W0(e) cat(w0[e])
#define W1(e) cat(w1[e])
#define TIEW(wc) TIE4(wc[0].lo, wc[0].hi, wc[1].lo, wc[1].hi); TIE4(wc[2].lo, wc[2].hi, wc[3].lo, wc[3].hi)
#define RW(wn, e, dlo, dhi, KO) DTR(wn[e].lo, dlo, KO); DTR(wn[e].hi, dhi, KO)
#define KD(BC, TIEB, wn, zc, zn, KO, SYNC, X10, X11, X12, X14, X15, S) \
    TIEB; TIE2(fa[0], fa[1]); TIE1(zc); WL(1); SCHED(); \
    G4(0, fa[0], BC, S(0), S(1), SYNC; S(2), RW(wn, 0, d000, d100, KO); S(3)); \
    G4(1, fa[1], BC, RW(wn, 1, d001, d101, KO); S(4), DSR(fa[0], hD, KO); S(5), RW(wn, 2, d010, d110, KO); S(6), \
       RW(wn, 3, d011, d111, KO); TIE1(fa[2]); WL(9); S(7)); \
    G4(2, fa[2], BC, DSR(zn, hD, KO + 3072); S(8), DSR(fa[1], hD, KO + 1024); S(9), X10; S(10), X11; S(11)); \
    G4(3, zc, BC, X12; S(12), DSR(fa[2], hD, KO + 2048); S(13), X14; S(14), X15; S(15));
#define KD3(BC, wc, wn, zc, zn, KO, SYNC, X0, X8, X9, X10, X11, H0, H1, H2, S) \
    TIEF(wc[0]); TIEF(wc[1]); TIE1(fa[1]); WL(H0); SCHED(); \
    G4(1, fa[1], BC, X0; S(0), TIEF(wc[2]); WL(H1); S(1), TIEF(wc[3]); WL(H2); SYNC; S(2), RW(wn, 0, d000, d100, KO); TIE1(fa[2]); TIE1(zc); WL(2); S(3)); \
    G4(2, fa[2], BC, RW(wn, 1, d001, d101, KO); S(4), DSR(fa[1], hD, KO + 1024); S(5), RW(wn, 2, d010, d110, KO); S(6), RW(wn, 3, d011, d111, KO); S(7)); \
    G4(3, zc, BC, DSR(zn, hD, KO + 3072); X8, DSR(fa[2], hD, KO + 2048); X9, X10, X11);
#define NEXT_SLOT() dl = slot == 4 ? -4 * 32768 : 32768; slot = slot == 4 ? 0 : slot + 1
  float gt[4];
  auto gop = [&](auto N) {
    constexpr int n = decltype(N)::v;
    if constexpr (n < 4) gt[n] = c2 ? cz[n ^ 2] : cz[n];
    else if constexpr (n < 8) gt[n - 4] = __builtin_amdgcn_rcpf(gt[n - 4]);
    else if constexpr (n < 12) gt[n - 8] = 1.f - gt[n - 8];
  };
  auto sop = [&](auto N) {
    constexpr int n = decltype(N)::v - 8;
    if constexpr (n >= 0 && n < 16) acc[n >> 2][n & 3] *= gt[n >> 2];
  };
#define Q1(n) gop(IC<(n)>{})
#define Q2(n) gop(IC<(n)>{}); gop(IC<(n) + 1>{})
#define SN(g)
#define SA(g) sop(IC<(g)>{})
#define SB(g) sop(IC<16 + (g)>{})
#define ADV_D ADV(d000, dl); ADV(d100, dl), ADV(d001, dl); ADV(d101, dl), ADV(d010, dl); ADV(d110, dl), ADV(d011, dl); ADV(d111, dl)
#define KD_(...) KD(__VA_ARGS__)
#define KD3_(...) KD3(__VA_ARGS__)
  int dl;
  KC(FB0, fb0, fb1, fz0, fz1, aC1, 8192, , , , Q1, Q2, 0);
  TF wa[4], wb[4], wc[4], wd[4];
  bf16x8 s0, s1, s2, s3;
#define WA(e) cat(wa[e])
#define WB(e) cat(wb[e])
#define WC(e) cat(wc[e])
#define WD(e) cat(wd[e])
#define ADV_DS(sn) ADV(d000, dl); ADV(d100, dl), ADV(d001, dl); ADV(d101, dl), DSR(sn, hD, 8192); ADV(d010, dl); ADV(d110, dl), ADV(d011, dl); ADV(d111, dl)
  KD(FB1, TIE4(fb1[0], fb1[1], fb1[2], fb1[3]), wa, fz1, fz0, 0, WL(0); BAR(), , , , , , SA);
  TIE4(acc[0][0], acc[0][1], acc[0][2], acc[0][3]);
  s0 = fa[0];
  STAMP(16);
  NEXT_SLOT();
  KD3_(WA, wa, wb, fz0, fz1, 8192, , , ADV_DS(s1), 1, 1, 1, SB);
  KD3(WB, wb, wc, fz1, fz0, 0, WL(0); BAR(), ADV(hD, dl), , , DSR(s2, hD, 0), , 7, 5, 3, SN);
  NEXT_SLOT();
  KD3_(WC, wc, wd, fz0, fz1, 8192, , , ADV_DS(s3), 7, 5, 3, SN);
  KD3(WD, wd, w0, fz1, fz0, 0, WL(0); BAR(), ADV(hD, dl), , , , , 7, 5, 3, SN);
#pragma unroll 1
  for (int t = 2; t < 6; ++t) {
    NEXT_SLOT();
    KD3_(W0, w0, w1, fz0, fz1, 8192, , , ADV_D, 6, 4, 2, SN);
    KD3(W1, w1, w0, fz1, fz0, 0, WL(0); BAR(), ADV(hD, dl), , , , , 6, 4, 2, SN);
  }
  NEXT_SLOT();
  KD3_(W0, w0, w1, fz0, fz1, 8192, , , ADV_D, 6, 4, 2, SN);
  u32x4 pq;
  auto xop = [&](auto N) {
    constexpr int n = decltype(N)::v, i = 1 + n / 18, h = (n / 9) & 1, o = n % 9;
    if constexpr (o < 4) pq[o] = pk_bf16(acc[i][2 * h + (o >> 1)][2 * (o & 1)], acc[i][2 * h + (o >> 1)][2 * (o & 1) + 1]);
    else if constexpr (o < 8) pq[o - 4] = min(pq[o - 4], 0xfffffffeu);
    else st_wt(pq, rp, lane * 16 + h * 1024, (((s ^ i) * 3 + (i - 1)) * 4 + w) * 2048);
  };
#define X3(n) xop(IC<(n)>{}); xop(IC<(n) + 1>{}); xop(IC<(n) + 2>{})
  TF w2[4];
#define W2(e) cat(w2[e])
  TIEF(w1[0]); TIEF(w1[1]); TIE1(fa[1]); WL(6); SCHED();
  G4(1, fa[1], W1, ADV(hD, dl), TIEF(w1[2]); WL(4), TIEF(w1[3]); TIE1(fa[2]); TIE1(fz1); WL(0), RW(w0, 0, d000, d100, 0); RW(w0, 1, d001, d101, 0));
  G4(2, fa[2], W1, RW(w0, 2, d010, d110, 0); RW(w0, 3, d011, d111, 0), DSR(fa[0], hD, 0); RW(w2, 0, d000, d100, 8192), RW(w2, 1, d001, d101, 8192); X3(0), RW(w2, 2, d010, d110, 8192); X3(3));
  G4(3, fz1, W1, RW(w2, 3, d011, d111, 8192); X3(6), DSR(fz0, hD, 8192); X3(9), X3(12), X3(15));
  STAMP(17);
  NEXT_SLOT();
  X3(18); X3(21); X3(24); X3(27); X3(30); X3(33); SCHED();
  X3(36); ADV(d000, dl); ADV(d100, dl); X3(39); ADV(d001, dl); ADV(d101, dl); X3(42); ADV(d010, dl); ADV(d110, dl);
  X3(45); ADV(d011, dl); ADV(d111, dl); X3(48); ADV(hD, dl); X3(51); SCHED();
  RW(w1, 0, d000, d100, 0); RW(w1, 1, d001, d101, 0); RW(w1, 2, d010, d110, 0); RW(w1, 3, d011, d111, 0); DSR(fa[1], hD, 0);
  u32x4 pa[6], pb[6];
  const int pva = lane * 16, po0 = (s * 12 + w) * 2048, po1 = po0 + 8192, po2 = po0 + 16384;
#define XIN [va] "v"(pva), [rp] "s"(rp), [o0] "s"(po0), [o1] "s"(po1), [o2] "s"(po2)
#define LDA "buffer_load_dwordx4 v[100:103], %[va], %[rp], %[o0] offen sc1\n\t" \
    "buffer_load_dwordx4 v[104:107], %[va], %[rp], %[o0] offen offset:1024 sc1\n\t" \
    "buffer_load_dwordx4 v[108:111], %[va], %[rp], %[o1] offen sc1\n\t" \
    "buffer_load_dwordx4 v[112:115], %[va], %[rp], %[o1] offen offset:1024 sc1\n\t" \
    "buffer_load_dwordx4 v[116:119], %[va], %[rp], %[o2] offen sc1\n\t" \
    "buffer_load_dwordx4 v[120:123], %[va], %[rp], %[o2] offen offset:1024 sc1\n\t"
#define LDB "buffer_load_dwordx4 v[124:127], %[va], %[rp], %[o0] offen sc1\n\t" \
    "buffer_load_dwordx4 v[128:131], %[va], %[rp], %[o0] offen offset:1024 sc1\n\t" \
    "buffer_load_dwordx4 v[132:135], %[va], %[rp], %[o1] offen sc1\n\t" \
    "buffer_load_dwordx4 v[136:139], %[va], %[rp], %[o1] offen offset:1024 sc1\n\t" \
    "buffer_load_dwordx4 v[140:143], %[va], %[rp], %[o2] offen sc1\n\t" \
    "buffer_load_dwordx4 v[144:147], %[va], %[rp], %[o2] offen offset:1024 sc1\n\t"
#define CKA "v_max3_u32 %[t], v100, v101, v102\n\t" \
    "v_max3_u32 %[t], %[t], v103, v104\n\t" \
    "v_max3_u32 %[t], %[t], v105, v106\n\t" \
    "v_max3_u32 %[t], %[t], v107, v108\n\t" \
    "v_max3_u32 %[t], %[t], v109, v110\n\t" \
    "v_max3_u32 %[t], %[t], v111, v112\n\t" \
    "v_max3_u32 %[t], %[t], v113, v114\n\t" \
    "v_max3_u32 %[t], %[t], v115, v116\n\t" \
    "v_max3_u32 %[t], %[t], v117, v118\n\t" \
    "v_max3_u32 %[t], %[t], v119, v120\n\t" \
    "v_max3_u32 %[t], %[t], v121, v122\n\t" \
    "v_max_u32 %[t], %[t], v123\n\t" \
    "v_cmp_eq_u32 vcc, -1, %[t]\n\t"
#define CKB "v_max3_u32 %[t], v124, v125, v126\n\t" \
    "v_max3_u32 %[t], %[t], v127, v128\n\t" \
    "v_max3_u32 %[t], %[t], v129, v130\n\t" \
    "v_max3_u32 %[t], %[t], v131, v132\n\t" \
    "v_max3_u32 %[t], %[t], v133, v134\n\t" \
    "v_max3_u32 %[t], %[t], v135, v136\n\t" \
    "v_max3_u32 %[t], %[t], v137, v138\n\t" \
    "v_max3_u32 %[t], %[t], v139, v140\n\t" \
    "v_max3_u32 %[t], %[t], v141, v142\n\t" \
    "v_max3_u32 %[t], %[t], v143, v144\n\t" \
    "v_max3_u32 %[t], %[t], v145, v146\n\t" \
    "v_max_u32 %[t], %[t], v147\n\t" \
    "v_cmp_eq_u32 vcc, -1, %[t]\n\t"
#define GETA() asm volatile(LDA : "=&{v[100:103]}"(pa[0]), "=&{v[104:107]}"(pa[1]), "=&{v[108:111]}"(pa[2]), "=&{v[112:115]}"(pa[3]), "=&{v[116:119]}"(pa[4]), "=&{v[120:123]}"(pa[5]) : XIN : "memory"); SCHED()
#define OWN(WC, wc, fc, wp, fp, KO, NW, A2_, A3_) \
    TIEW(wc); TIE1(fc); WL(NW); SCHED(); \
    G4(0, fc, WC, RW(wp, 0, d000, d100, KO); RW(wp, 1, d001, d101, KO), RW(wp, 2, d010, d110, KO); RW(wp, 3, d011, d111, KO), DSR(fp, hD, KO); A2_, A3_);
#define OWN_ADV NEXT_SLOT(); ADV(d000, dl); ADV(d100, dl); ADV(d001, dl); ADV(d101, dl), ADV(d010, dl); ADV(d110, dl); ADV(d011, dl); ADV(d111, dl); ADV(hD, dl)
#define OWN_(...) OWN(__VA_ARGS__)
  TIE4(s0, s1, s2, s3); TIE4(acc[0][0], acc[0][1], acc[0][2], acc[0][3]); SCHED();
  G4(0, s0, WA, , , , ); G4(0, s1, WB, , , , ); G4(0, s2, WC, , , , ); G4(0, s3, WD, , , , );
  TIEW(w0); TIE1(fa[0]); WL(15); SCHED();
  G4(0, fa[0], W0, , , , );
  OWN_(W2, w2, fz0, w0, fa[0], 8192, 9, OWN_ADV);
  OWN(W1, w1, fa[1], w2, fz0, 0, 9, , );
  OWN_(W0, w0, fa[0], w1, fa[1], 8192, 9, OWN_ADV);
  OWN(W2, w2, fz0, w0, fa[0], 0, 9, , );
  OWN_(W1, w1, fa[1], w2, fz0, 8192, 9, OWN_ADV);
  OWN(W0, w0, fa[0], w1, fa[1], 0, 9, , );
  OWN(W2, w2, fz0, w0, fa[0], 8192, 9, , );
  TIEW(w1); TIE1(fa[1]); WL(9); SCHED();
  G4(0, fa[1], W1, , , , );
  TIEW(w0); TIE1(fa[0]); WL(0); SCHED();
  G4(0, fa[0], W0, , , , );
  STAMP(18);
  auto fin = [&](const u32x4 (&pv)[6]) {
    STAMP(19);
#pragma unroll
    for (int k = 0; k < 6; ++k)
#pragma unroll
      for (int d = 0; d < 4; ++d) {
        const int e = 2 * (k & 1) + (d >> 1), r = 2 * (d & 1);
        acc[0][e][r] += __builtin_bit_cast(float, pv[k][d] << 16);
        acc[0][e][r + 1] += __builtin_bit_cast(float, pv[k][d] & 0xffff0000u);
      }
    u16* op = out + (size_t)(rb * 128 + (4 * r2 + s) * 16 + rho) * D_ + nb * 128 + 64 * c2 + 8 * q;
#pragma unroll
    for (int h = 0; h < 2; ++h) {
      const unsigned a0 = pk_bf16(acc[0][2 * h][0], acc[0][2 * h][1]), a1 = pk_bf16(acc[0][2 * h][2], acc[0][2 * h][3]);
      const unsigned b0 = pk_bf16(acc[0][2 * h + 1][0], acc[0][2 * h + 1][1]), b1 = pk_bf16(acc[0][2 * h + 1][2], acc[0][2 * h + 1][3]);
      const bool o = q & 1;
      const u32x4 v = {o ? b0 : a0, o ? b1 : a1, o ? a0 : b0, o ? a1 : b1};
      *(u32x4*)(op + 32 * h) = v;
    }
  };
  for (unsigned spins = 0; spins < (1u << 16); ++spins) {
#pragma unroll
    for (int k = 0; k < 6; ++k) pa[k] = __builtin_amdgcn_raw_buffer_load_b128(rp, lane * 16 + (k & 1) * 1024, ((s * 3 + (k >> 1)) * 4 + w) * 2048, 16);
    unsigned m = 0;
#pragma unroll
    for (int k = 0; k < 6; ++k) m = max(max(m, max(pa[k][0], pa[k][1])), max(pa[k][2], pa[k][3]));
    if (__all(m != 0xffffffffu)) break;
    asm volatile("" ::: "memory");
  }
  fin(pa);
  if (CODEW) asm volatile(".Lkea:");
  STAMPR(20);
  return;
  }
  {
    LANE(lane);
    const int lw = wid - 4, hand = lw >> 1, l1 = lw & 1;
    const int r8 = lane >> 3, c8 = lane & 7, row4 = lane >> 4, ph = lane & 15;
    const int sw0 = (c8 ^ (r8 >> 1)) * 16, sw1 = sw0 ^ 64;
    const __amdgpu_buffer_rsrc_t rx = mk_rsrc(x + (size_t)rb * 128 * D_, 128 * 1024);
    unsigned c0 = 0, c1 = 0;
      const size_t tco = (size_t)rb * 128 * T_ + s * 512;
      const unsigned tn = (unsigned)(((size_t)T_ * T_ - tco) * 2);
      const int tv = (32 * lw + (lane & 31)) * (T_ * 2) + ((j + rb) & 3) * 128, tv2 = tv + (lane >> 5) * 512;
#define TC(g) do { if (lane < 32) __builtin_amdgcn_raw_ptr_buffer_load_lds(mk_rsrc(((g) < 2 ? Cr : Ci) + tco, tn), (LAS void*)(smem + L_TCH + lw * 256), 4, tv, ((g) & 1) * 512, 0, 0); } while (0)
    const int tvq = (32 * lw + (lane & 15)) * (T_ * 2) + ((j + rb) & 3) * 128;
#define TQ(k) do { if (lane < 16) __builtin_amdgcn_raw_ptr_buffer_load_lds(mk_rsrc(((k) < 4 ? Cr : Ci) + tco, tn), (LAS void*)(smem + L_TCH + lw * 256), 4, tvq, ((k) >> 1 & 1) * 512 + ((k) & 1) * 65536, 0, 0); } while (0)
    unsigned td2 = 0, td3 = 0;
#define TCV(d, g) do { if (lane < 32) d = __builtin_amdgcn_raw_buffer_load_b32(mk_rsrc(((g) < 2 ? Cr : Ci) + tco, tn), tv, ((g) & 1) * 512, 0); } while (0)
#define TC2(g) __builtin_amdgcn_raw_ptr_buffer_load_lds(mk_rsrc(((g) < 2 ? Cr : Ci) + tco, tn), (LAS void*)(smem + L_TCH + lw * 256), 4, tv2, 0, 0, 0)
    {
      const __amdgpu_buffer_rsrc_t rh = mk_rsrc((cb < 8 ? Hr : Hi) + (size_t)(cb & 7) * 64 * D_, 64 * 1024);
      const int rx_ = (8 * (r8 >> 2) + (r8 & 3)) * 1024;
      const int rh_ = (32 * (lw >> 1) + 8 * (r8 >> 2) + 4 * (l1 ^ (r8 >> 2)) + (r8 & 3)) * 1024;
      const int vxa[2] = {rx_ + sw0, rx_ + sw1}, vha[2] = {rh_ + sw0, rh_ + 16384 + sw1};
      auto issueA = [&](int t, int sl) {
        LAS char* sb = smem + sl * 24576;
        const int ko = UN(AORD, off, t) * 128;
#pragma unroll
        for (int i = 0; i < 4; ++i) glds<0>(rx, sb + (4 * lw + i) * 1024, vxa[i & 1], (32 * lw + 16 * (i & 1) + 4 * (i >> 1)) * 1024 + ko);
#pragma unroll
        for (int i = 0; i < 2; ++i) glds<1>(rh, sb + (sl == 4 ? 57344 : 16384) + (2 * lw + i) * 1024, vha[i], ko);
      };
      if (lw == 0) glds<1>(mk_rsrc(gwp, 1024), smem + L_GW, lane * 16, 0);
      issueA(0, 0); issueA(1, 1); issueA(2, 2);
      if (CORD >= 6) {
        const int c = CORD == 6 ? rb & 3 : (rb + s) & 3, r = j + (rb >> 2);
#pragma unroll
        for (int t = 15; t >= 0; --t) {
          const int tt = t - c, g = t < 4 ? c : 4 - c, e = (t < 4 ? t : tt - 12) + j;
          const int em = g == 3 ? (e >= 3 ? e - 3 : e) : g == 2 ? e & 1 : 0;
          const int u = (t < 3 && tt < 0) ? em : (t >= 13 && c && tt >= 12) ? 12 + c + em : c + (tt & 12) + ((tt + r) & 3);
          if (t >= 8) c1 = c1 << 4 | (unsigned)u; else c0 = c0 << 4 | (unsigned)u;
        }
        c0 = __builtin_amdgcn_readfirstlane(c0); c1 = __builtin_amdgcn_readfirstlane(c1);
        asm volatile("" : "+s"(c0), "+s"(c1));
      }
      const int tbx0 = (2 * lw & 4) | ((2 * lw & 3) ^ s), tbx1 = tbx0 ^ 1;
      const __amdgpu_buffer_rsrc_t rw = mk_rsrc(hand ? Wu : Wg, D_ * F_ * 2);
      const int vxb[2] = {r8 * 1024 + sw0, r8 * 1024 + 8192 + sw1};
      int vw[2];
#pragma unroll
      for (int hb = 0; hb < 2; ++hb) {
        const int v = ph ^ ((row4 & 1) | ((row4 >> 1) << 2) | (hb << 3));
        int gu = v + 2 * (cb & 1); gu = gu >= 14 ? gu - 14 : gu;
        vw[hb] = v < 14 ? row4 * (F_ * 2) + cb * 224 + gu * 16 : (int)0x80000000;
      }
      auto issueBX = [&](int t, int reg) {
        LAS char* sb = smem + reg * 49152;
        t = UN(BORD, offb, t);
#pragma unroll
        for (int i = 0; i < 4; ++i) glds<2>(rx, sb + (4 * lw + i) * 1024, vxb[i & 1], ((i >> 1) ? tbx1 : tbx0) * 16384 + t * 128);
      };
      auto issueBW = [&](int t, int reg) {
        LAS char* sb = smem + reg * 49152;
        t = UN(BORD, offb, t);
#pragma unroll
        for (int i = 0; i < 8; ++i) glds<3>(rw, sb + 16384 + (8 * lw + i) * 1024, vw[(i >> 1) & 1], (64 * t + 32 * l1 + 4 * i) * (F_ * 2));
      };
      auto issueB = [&](int t, int reg) { issueBX(t, reg); issueBW(t, reg); };
      WAIT_V(12); BAR();
      issueA(3, 3); issueA(4, 4);
      WAIT_V(18); BAR(); issueA(5, 0); issueBW(0, 2);
      WAIT_V(26); BAR();
      WAIT_V(20); BAR(); issueA(6, 2);
      WAIT_V(20); BAR(); issueA(7, 3);
      WAIT_V(20); BAR(); issueBX(0, 2);
      WAIT_V(10); BAR(); issueB(1, 0);
      WAIT_V(16); BAR();
      WAIT_V(12); BAR(); issueB(2, 1);
      WAIT_V(12); BAR(); issueB(3, 2); TC(0);
      WAIT_V(13); BAR(); issueB(4, 0);
      WAIT_V(13); BAR(); issueB(5, 1); TC(1);
      WAIT_V(13); BAR(); issueB(6, 2);
      WAIT_V(13); BAR();
      unsigned pv = 1u;
      const int fa = (((4 * s + ((lane >> 2) & 3)) * 16 + 2 * nb + ((lane >> 4) & 1) + 8 * (lane >> 5)) * 16 + (lane & 3)) * 4;
      if (hand) asm volatile("global_load_dword %0, %1, %2 sc1" : "=v"(pv) : "v"(fa), "s"(ws) : "memory");
      issueB(7, 0);
      asm volatile("s_waitcnt vmcnt(12)" : "+v"(pv)::"memory"); BAR();
      if (hand) {
        STAMP(2);
        if (!__all(pv != 0u)) {
          for (unsigned spins = 0; spins < (1u << 16); ++spins) {
            if (__all(__hip_atomic_load((GAS unsigned*)(ws + fa), __ATOMIC_RELAXED, __HIP_MEMORY_SCOPE_AGENT) != 0u)) break;
            __builtin_amdgcn_s_sleep(1);
          }
        }
        INV();
        STAMP(3);
      }
    }
    int slot = 2;
    {
      const size_t coff = (size_t)rb * 128 * T_ + s * 512;
      const char* abf = ws + WS_ABF + (size_t)(s * 4 + nb) * 262144;
      const unsigned n0 = hand ? 262144u : (unsigned)(((size_t)T_ * T_ - coff) * 2), n1 = hand ? 131072u : n0;
      const __amdgpu_buffer_rsrc_t r0 = mk_rsrc(hand ? abf : (const char*)(Cr + coff), n0);
      const __amdgpu_buffer_rsrc_t r1 = mk_rsrc(hand ? abf + 131072 : (const char*)(Ci + coff), n1);
      const int vo[2] = {hand ? lane * 16 : r8 * (T_ * 2) + sw0, hand ? lane * 16 : r8 * (T_ * 2) + sw1};
      const int inc = hand ? 16384 : 128;
      int ro[8];
#pragma unroll
      for (int i = 0; i < 8; ++i) {
        const int P = 4 * l1 + (i >> 1), tb = (P & 4) | ((P & 3) ^ s);
        ro[i] = hand ? (8 * l1 + i) * 1024 : (16 * tb + 8 * (i & 1)) * (T_ * 2);
      }
      auto issueC = [&](int t) {
        LAS char* sb = smem + slot * 32768 + lw * 8192;
        const int p2 = 2 * (rb & 1), tt = t - p2;
        const int un = CORD >= 6 ? (int)((t < 8 ? c0 : c1) >> (4 * (t & 7))) & 15 : CORD == 5 ? ((p2 && (t < 2 || t >= 14)) ? (t & 14) + ((t + j) & 1) : p2 + (tt & 12) + ((tt + j + (rb >> 1)) & 3)) : CORD == 1 ? 4 * (((t >> 2) + rb) & 3) + ((t + j) & 3) : CORD == 2 ? 4 * (t >> 2) + ((t + j + rb) & 3)
                     : CORD == 3 ? 4 * (((t >> 2) + (rb & 1)) & 1) + (t & 8) + ((t + j + (rb >> 1)) & 3) : CORD == 4 ? (((t >> 3) + rb) & 1) * 8 + ((t + offc) & 7) : (t & 8) + ((t + offc) & 7);
        const __amdgpu_buffer_rsrc_t r = un < 8 ? r0 : r1;
        if (hand) {
#pragma unroll
          for (int i = 0; i < 8; ++i) glds1(r, sb + i * 1024, vo[i & 1], ro[i] + (un & 7) * inc);
        } else {
#pragma unroll
          for (int i = 0; i < 8; ++i) glds<4>(r, sb + i * 1024, vo[i & 1], ro[i] + (un & 7) * inc);
        }
        slot = slot == 4 ? 0 : slot + 1;
      };
      issueC(0);
      WAIT_V(8); BAR(); issueC(1); issueC(2);
      WAIT_V(16); BAR(); issueC(3); issueC(4);
#pragma unroll 1
      for (int t = 5; t < 15; ++t) { WAIT_V(24); BAR(); issueC(t); }
      WAIT_V(24); BAR();
      unsigned pv = 1u;
      const int pl = lane < 28 ? lane : 0;
      const int fb = (pl < 16 ? (rb * 16 + 4 * s + (pl >> 2)) * 16 + 4 + (pl & 3) : (rb * 16 + 4 * (s ^ ((pl >> 2) - 3)) + j) * 16 + 4 + (pl & 3)) * 4;
      if (hand) asm volatile("global_load_dword %0, %1, %2 sc1" : "=v"(pv) : "v"(fb), "s"(ws) : "memory");
      issueC(15);
      WAIT_V(24); BAR();
      if (hand) {
        asm volatile("s_waitcnt vmcnt(8)" : "+v"(pv)::"memory");
        STAMP(4);
        if (!__all(pv != 0u)) {
          for (unsigned spins = 0; spins < (1u << 16); ++spins) {
            if (__all(__hip_atomic_load((GAS unsigned*)(ws + fb), __ATOMIC_RELAXED, __HIP_MEMORY_SCOPE_AGENT) != 0u)) break;
            __builtin_amdgcn_s_sleep(1);
          }
        }
        INV();
        STAMP(5);
      }
    }
    {
      const size_t woff = (size_t)448 * s * D_ + nb * 128;
      const __amdgpu_buffer_rsrc_t r = mk_rsrc(hand ? ws + WS_HF + (size_t)(rb * 4 + s) * 114688 : (const char*)(Wd + woff),
                                               hand ? 114688u : (unsigned)(((size_t)F_ * D_ - woff) * 2));
      const int u0 = ph ^ ((row4 & 1) | ((row4 >> 1) << 2));
      const int vo[2] = {hand ? lane * 16 : row4 * 1024 + u0 * 16, hand ? lane * 16 : row4 * 1024 + (u0 ^ 8) * 16};
      const int inc = hand ? 16384 : 65536;
      int ro[8];
#pragma unroll
      for (int i = 0; i < 8; ++i) ro[i] = hand ? l1 * 8192 + ((i & 4) | ((i & 3) ^ s)) * 1024 : (32 * l1 + 4 * i) * 1024;
      const int offd = DROT == 1 ? rb % 7 : DROT == 2 ? (rb + 2 * j) % 7 : DROT == 3 ? (rb + j) % 7 : 0;
      const int o3 = (j + rb) % 3;
      auto issueD = [&](int t) {
        LAS char* sb = smem + slot * 32768 + (lw ^ 2) * 8192;
        if (DROT == 4) { if (t < 4) t = (t + j + rb) & 3; else { t += o3; t = t >= 7 ? t - 3 : t; } }
        else { t += offd; t = t >= 7 ? t - 7 : t; }
        if (hand) {
#pragma unroll
          for (int i = 0; i < 8; ++i) glds1(r, sb + i * 1024, vo[(i >> 1) & 1], ro[i] + t * inc);
        } else {
#pragma unroll
          for (int i = 0; i < 8; ++i) glds<5>(r, sb + i * 1024, vo[(i >> 1) & 1], ro[i] + t * inc);
        }
        slot = slot == 4 ? 0 : slot + 1;
      };
      issueD(0);
#pragma unroll 1
      for (int t = 1; t < 7; ++t) { WAIT_V(24); BAR(); issueD(t); }
      WAIT_V(24); BAR();
      WAIT_V(16); BAR();
      WAIT_V(8); BAR();
      WAIT_V(0); STAMP(6); BAR();
      asm volatile("" ::"v"(td2), "v"(td3));
      if (CODEW) asm volatile(".Lkeb:");
    }
  }

}

#define PREP 0

extern "C" void kernel_launch(void* const* d_in, const int* in_sizes, int n_in, void* d_out, int out_size, void* d_ws,
                              size_t ws_size, hipStream_t stream) {
  (void)hipMemsetAsync((char*)d_ws + WS_FLAG, 0, 16384, stream);
  fused<<<256, 512, 0, stream>>>((const u16*)d_in[0], (const u16*)d_in[1], (const u16*)d_in[2], (const u16*)d_in[3], (const u16*)d_in[4],
                                 (const u16*)d_in[6], (const u16*)d_in[7], (const u16*)d_in[8], (const u16*)d_in[9], (char*)d_ws, (u16*)d_out);
}
```
